```python
import torch

def _softmax(x, axis=-1):
    return torch.softmax(x, dim=axis)
def _gelu(x):
    return torch.nn.functional.gelu(x, approximate="tanh")
def _silu(x):
    return torch.nn.functional.silu(x)
def _sigmoid(x):
    return torch.sigmoid(x)
def _layernorm(x, axis=-1, eps=1e-5):
    m = x.mean(dim=axis, keepdim=True)
    v = x.var(dim=axis, keepdim=True, unbiased=False)
    return (x - m) / torch.sqrt(v + eps)
def _scatter_add(tgt, idx, src, axis=0):
    return tgt.clone().index_add_(axis, idx, src)
def _segment_sum(src, seg, n):
    out = torch.zeros((n,) + src.shape[1:], dtype=src.dtype)
    return out.index_add_(0, seg, src)
def _pad(x, pw):
    flat = tuple(v for pair in reversed(pw) for v in pair)
    return torch.nn.functional.pad(x, flat)

kernel_name = 'gated_mix__linear_attn_block__dense_activation_squared_relu__prompt'

def setup_inputs():

    _w = (lambda a: (lambda t: t.to(torch.bfloat16) if t.is_floating_point() else t)(torch.as_tensor(a)))
    def _A_setup():
        B, S, D = 1, 2048, 512
        return {
            "x": _w((lambda *s: torch.randn(*s, dtype=torch.bfloat16) * 0.05)(B, S, D)),
            "wqkv": _w((lambda *s: torch.randn(*s, dtype=torch.bfloat16) * 0.8)(D, 3 * D)),
            "wo": _w((lambda *s: torch.randn(*s, dtype=torch.bfloat16) * 0.2)(D, D)),
        }
    _A_in = _A_setup()

    def _B_setup():
        B, S, D, F = 1, 2048, 512, 1792
        return {
            "x": _w((lambda *s: torch.randn(*s, dtype=torch.bfloat16) * 0.05)(B, S, D)),
            "w1": _w((lambda *s: torch.randn(*s, dtype=torch.bfloat16) * 0.1)(D, F)),
            "b1": _w((lambda *s: torch.randn(*s, dtype=torch.bfloat16) * 0.05)(F)),
            "w2": _w((lambda *s: torch.randn(*s, dtype=torch.bfloat16) * 0.05)(F, D)),
            "b2": _w((lambda *s: torch.randn(*s, dtype=torch.bfloat16) * 0.05)(D)),
        }
    _B_in = _B_setup()

    return {
        "A_x": _A_in["x"],
        "A_wqkv": _A_in["wqkv"],
        "A_wo": _A_in["wo"],
        "B_x": _B_in["x"],
        "B_w1": _B_in["w1"],
        "B_b1": _B_in["b1"],
        "B_w2": _B_in["w2"],
        "B_b2": _B_in["b2"],
        "gate_w": _w((lambda *s: torch.randn(*s, dtype=torch.bfloat16) * 0.05)(512) * 16.0),
    }

def reference(A_x, A_wqkv, A_wo, B_x, B_w1, B_b1, B_w2, B_b2, gate_w):
    def _A_ref(x, wqkv, wo):
        B, S, D = x.shape
        H = 32; Dh = D // H
        qkv = (lambda x, *d: x.permute(*d))((x @ wqkv).reshape(B, S, 3, H, Dh), 2, 0, 3, 1, 4)
        q, k, v = qkv[0], qkv[1], qkv[2]
        phi = lambda t: torch.where(t > 0, t + 1, torch.exp(t)) + 1e-6
        pq, pk = phi(q), phi(k)
        kv = torch.einsum("bhsd,bhse->bhde", pk, v)
        z = torch.einsum("bhsd->bhd", pk)
        num = torch.einsum("bhqd,bhde->bhqe", pq, kv)
        den = torch.einsum("bhqd,bhd->bhq", pq, z)[..., None] + 1e-6
        o = num / den
        return (lambda x, *d: x.permute(*d))(o, 0, 2, 1, 3).reshape(B, S, D) @ wo

    def _B_ref(x, w1, b1, w2, b2):
        h = torch.einsum("bsd,df->bsf", x, w1) + b1
        zero = torch.zeros_like(h)
        r = torch.where(h > zero, h, zero)
        h = r * r
        out = torch.einsum("bsf,fd->bsd", h, w2) + b2
        return out

    x = A_x
    g = _sigmoid((x * gate_w).sum(-1, keepdims=True))
    a = _A_ref(x, A_wqkv, A_wo)
    b = _B_ref(x, B_w1, B_b1, B_w2, B_b2)
    return g * a + (1 - g) * b
```

```cpp
#include <hip/hip_runtime.h>
#include <hip/hip_bf16.h>
typedef unsigned short u16;
__device__ __forceinline__ float bf2f(u16 v){ return __uint_as_float(((unsigned)v)<<16); }
__device__ __forceinline__ u16 f2bf(float f){ unsigned u=__float_as_uint(f); u += 0x7fff + ((u>>16)&1); return (u16)(u>>16); }
__global__ void gemm(const u16* A, const u16* B, float* C, int M, int N, int K){
  int n = blockIdx.x*64 + (threadIdx.x&63); int m = blockIdx.y*4 + (threadIdx.x>>6);
  float s=0; for(int k=0;k<K;k++) s += bf2f(A[m*K+k])*bf2f(B[k*N+n]);
  C[m*N+n]=s;
}
__device__ __forceinline__ float phi(float t){ return (t>0? t+1.f : __expf(t)) + 1e-6f; }
__global__ void kvk(const float* qkv, float* kv, float* z){
  int h=blockIdx.x, d=threadIdx.x>>4, e=threadIdx.x&15; float s=0, zz=0;
  for(int t=0;t<2048;t++){ float pk=phi(qkv[t*1536+512+h*16+d]); s+=pk*qkv[t*1536+1024+h*16+e]; zz+=pk; }
  kv[h*256+d*16+e]=s; if(e==0) z[h*16+d]=zz;
}
__global__ void ok(const float* qkv, const float* kv, const float* z, u16* o){
  int i=blockIdx.x*256+threadIdx.x; int t=i>>9, c=i&511, h=c>>4, e=c&15; float num=0, den=0;
  for(int d=0;d<16;d++){ float pq=phi(qkv[t*1536+h*16+d]); num+=pq*kv[h*256+d*16+e]; den+=pq*z[h*16+d]; }
  o[i]=f2bf(num/(den+1e-6f));
}
__global__ void hk(const float* hh, const u16* b1, u16* h){
  int i=blockIdx.x*256+threadIdx.x; int f=i%1792; float v=hh[i]+bf2f(b1[f]); v=v>0?v:0; h[i]=f2bf(v*v);
}
__global__ void fin(const u16* x, const u16* gw, const float* a, const float* b, const u16* b2, u16* out){
  int t=blockIdx.x; __shared__ float red[256]; float s=0;
  for(int c=threadIdx.x;c<512;c+=256) s+=bf2f(x[t*512+c])*bf2f(gw[c]);
  red[threadIdx.x]=s; __syncthreads();
  for(int o=128;o>0;o>>=1){ if(threadIdx.x<o) red[threadIdx.x]+=red[threadIdx.x+o]; __syncthreads(); }
  float g=1.f/(1.f+__expf(-red[0]));
  for(int c=threadIdx.x;c<512;c+=256) out[t*512+c]=f2bf(g*a[t*512+c]+(1.f-g)*(b[t*512+c]+bf2f(b2[c])));
}
extern "C" void kernel_launch(void* const* d_in, const int* in_sizes, int n_in, void* d_out, int out_size, void* d_ws, size_t ws_size, hipStream_t stream){
  const u16* x=(const u16*)d_in[0]; const u16* wqkv=(const u16*)d_in[1]; const u16* wo=(const u16*)d_in[2];
  const u16* w1=(const u16*)d_in[4]; const u16* b1=(const u16*)d_in[5]; const u16* w2=(const u16*)d_in[6]; const u16* b2=(const u16*)d_in[7]; const u16* gw=(const u16*)d_in[8];
  char* ws=(char*)d_ws; float* qkv=(float*)ws; float* hh=(float*)(ws+(16<<20)); float* kv=(float*)(ws+(32<<20)); float* z=kv+8192;
  u16* o=(u16*)(ws+(33<<20)); u16* h=(u16*)(ws+(36<<20)); float* a=(float*)(ws+(48<<20)); float* b=(float*)(ws+(56<<20));
  gemm<<<dim3(1536/64,512),256,0,stream>>>(x,wqkv,qkv,2048,1536,512);
  gemm<<<dim3(1792/64,512),256,0,stream>>>(x,w1,hh,2048,1792,512);
  kvk<<<32,256,0,stream>>>(qkv,kv,z);
  ok<<<4096,256,0,stream>>>(qkv,kv,z,o);
  hk<<<2048*1792/256,256,0,stream>>>(hh,b1,h);
  gemm<<<dim3(512/64,512),256,0,stream>>>(o,wo,a,2048,512,512);
  gemm<<<dim3(512/64,512),256,0,stream>>>(h,w2,b,2048,512,1792);
  fin<<<2048,256,0,stream>>>(x,gw,a,b,b2,(u16*)d_out);
}
```

```cpp
#include <hip/hip_runtime.h>
#include <utility>

typedef __attribute__((ext_vector_type(8))) __bf16 bf8_t;
typedef __attribute__((ext_vector_type(4))) __bf16 bf4_t;
typedef __attribute__((ext_vector_type(2))) __bf16 bf2_t;
typedef __attribute__((ext_vector_type(16))) float f16_t;
typedef __attribute__((ext_vector_type(4))) float f4_t;
typedef __attribute__((ext_vector_type(2))) float f2_t;
typedef __attribute__((ext_vector_type(4))) unsigned u4_t;
typedef __attribute__((ext_vector_type(2))) unsigned u2_t;
typedef __amdgpu_buffer_rsrc_t rsrc_t;
#define AS3 __attribute__((address_space(3)))
#define DI __device__ __forceinline__
#define SB() __builtin_amdgcn_sched_barrier(0)
#define MFMA(a, b, c) __builtin_amdgcn_mfma_f32_32x32x16_bf16((a), (b), (c), 0, 0, 0)
#define SC1 16

constexpr unsigned WS_KVP = 0x900000, WS_G = 0x980000, WS_HF = 0x9A0000, WS_KVF = 0x9A8000, WS_EP = 0x9B0000,
                   WS_PROF = 0xA00000, WS_END = 0xB00000;
constexpr unsigned SLOT = 16384, MISC = 9 * SLOT, L_GATE = MISC, L_B1 = MISC + 1024, L_B2 = MISC + 2048,
                   L_ZS = MISC + 2560, L_KVF = MISC + 4096, L_ZF = MISC + 6144, L_TAG = MISC + 6400, L_DUMP = MISC + 8192, L_KVS = 8 * SLOT, L_RED = 4 * SLOT;
constexpr int SPIN_MAX = 1 << 17;
#define PDIST 3
constexpr int NSTEP = 21;
constexpr int UPRO = 3;
constexpr int uafter(int t) {
  constexpr int U[NSTEP] = {5, 7, 9, 11, 13, 15, 17, 20, 23, 25, 27, 28, 30, 32, 34, 36, 38, 39, 39, 39, 39};
  return t < 0 ? UPRO : U[t];
}
constexpr int uneed(int t) { return t < 8 ? 2 * t + 1 : t == 8 ? 17 : t == 9 ? 18 : t == 10 ? 20 : t == 11 ? 21 : 23 + 2 * (t - 12); }
constexpr bool uskip(int type, int u) { return type == 0 && (u == 17 || u == 20); }
constexpr int vmwait(int type, int t) {
  int n = 0;
  for (int u = uneed(t) + 1; u <= uafter(t - 1); ++u) n += uskip(type, u) ? 0 : 4;
  return n;
}
constexpr unsigned slot(int u) { return (unsigned)(u % 9) * SLOT; }
constexpr unsigned slo(int u) { return slot(u) & 0xffffu; }
constexpr unsigned shi(int u) { return slot(u) & ~0xffffu; }
constexpr int ufull(int ks) { return ks < 8 ? 16 : ks < 16 ? 18 : ks < 24 ? 19 : 21; }

#define STAMP(id)

struct Params {
  const char *x, *wqkv, *wo, *w1, *b1, *w2, *b2, *gw;
  char *out, *ws;
};
struct Ids { int i, m, c, rh, rot; };

template <int N> DI void vmcnt() { asm volatile("s_waitcnt vmcnt(%0)" ::"n"(N) : "memory"); }
DI void bar() { asm volatile("s_waitcnt lgkmcnt(0)\n\ts_barrier" ::: "memory"); SB(); }
DI bf8_t lds128(unsigned a) { return *(AS3 bf8_t*)a; }
DI f4_t ldsf4(unsigned a) { return *(AS3 f4_t*)a; }
DI bf4_t ldstr(unsigned a) { return __builtin_amdgcn_ds_read_tr16_b64_v4bf16((AS3 bf4_t*)a); }
DI bf8_t cat(bf4_t a, bf4_t b) { return __builtin_shufflevector(a, b, 0, 1, 2, 3, 4, 5, 6, 7); }
DI unsigned pk(float a, float b) { f2_t f = {a, b}; return __builtin_bit_cast(unsigned, __builtin_convertvector(f, bf2_t)); }
DI float bflo(unsigned v) { return __builtin_bit_cast(float, v << 16); }
DI float bfhi(unsigned v) { return __builtin_bit_cast(float, v & 0xffff0000u); }
DI u4_t pk8(const f16_t& a, int s) { return u4_t{pk(a[8 * s], a[8 * s + 1]), pk(a[8 * s + 2], a[8 * s + 3]), pk(a[8 * s + 4], a[8 * s + 5]), pk(a[8 * s + 6], a[8 * s + 7])}; }
DI bf8_t asbf(u4_t v) { return __builtin_bit_cast(bf8_t, v); }
DI float relu(float t) { return __builtin_bit_cast(float, max(__builtin_bit_cast(int, t), 0)); }
DI float phi(float t) {
  float e = __builtin_amdgcn_fmed3f(__builtin_amdgcn_exp2f(t * 1.44269504088896f), 0.f, 1.f);
  return (relu(t) + e) + 1e-6f;
}
DI u4_t mkdesc(const void* p, unsigned bytes) {
  unsigned long long a = (unsigned long long)p;
  return u4_t{(unsigned)a, (unsigned)(a >> 32) & 0xffffu, bytes, 0x00020000u};
}
DI void dma(unsigned ldsa, unsigned voff, u4_t d, unsigned soff) {
  asm volatile("s_mov_b32 m0, %0\n\ts_nop 0\n\tbuffer_load_dwordx4 %1, %2, %3 offen lds" ::"s"(ldsa), "v"(voff), "s"(d), "s"(soff) : "memory");
}
DI unsigned ld32(rsrc_t r, unsigned v, unsigned s) { return __builtin_amdgcn_raw_buffer_load_b32(r, v, s, SC1); }
DI void waitflag(u4_t d, unsigned v, unsigned s, unsigned val, unsigned tag) {
  unsigned n = SPIN_MAX;
  asm volatile("s_nop 4\n1%=:\n\tv_cmp_ne_u32 vcc, %4, %0\n\ts_cbranch_vccz 2%=f\n\ts_sleep 1\n\tbuffer_load_dword %0, %2, %3, %5 offen sc1\n\t"
               "s_sub_u32 %1, %1, 1\n\ts_waitcnt vmcnt(0)\n\ts_cbranch_scc0 1%=b\n2%=:"
               : "+v"(val), "+s"(n) : "v"(v), "s"(d), "s"(tag), "s"(s) : "memory", "vcc", "scc");
}
template <class F, int... T> DI void sfor_(F&& f, std::integer_sequence<int, T...>) { (f(std::integral_constant<int, T>{}), ...); }
template <int N, class F> DI void sfor(F&& f) { sfor_(f, std::make_integer_sequence<int, N>{}); }
#define PIN(x) asm volatile("" : "+v"(x))
template <int FROM, int TO> DI void mv(unsigned& a) { if (shi(FROM) != shi(TO)) { a += shi(TO) - shi(FROM); PIN(a); } }
constexpr unsigned OOB = 0x80000000u;
DI int unit_tile(int u, int c) { return u < 8 ? 4 * ((u + c) & 7) : u < 14 ? 32 + 4 * ((u - 8 + c) % 6) : 56 + 4 * ((u - 14 + c) & 3); }

template <int TYPE> DI void loader(const Params& P, const Ids& I, int lw, int lane, rsrc_t ws, unsigned& prof) {
  constexpr unsigned WSTR = TYPE ? 3584 : 3072;
  const u4_t dx = mkdesc(P.x, 2048 * 1024), dw = TYPE ? mkdesc(P.w1, 512 * 3584) : mkdesc(P.wqkv, 512 * 3072);
  const u4_t d2 = mkdesc(P.w2, 1792 * 1024), dwo = mkdesc(P.wo, 512 * 1024);
  const unsigned r8 = lane >> 3, ch = lane & 7, sw = (ch ^ (((lane >> 4) & 1) << 2)) * 16;
  const unsigned vx0 = r8 * 1024 + (ch ^ (lane >> 4)) * 16, vx1 = r8 * 1024 + (ch ^ (4 + (lane >> 4))) * 16;
  const unsigned vw = r8 * WSTR + sw, vh = (lane >> 2) * WSTR + (lane & 3) * 16, v2 = r8 * 1024 + sw;
  const unsigned mo = I.m & 1;
  const unsigned xb = (I.i * 128 + lw * 32) * 1024;
  const unsigned c1 = TYPE ? 448 * I.m + 64 * mo + 128 * (lw >> 1) : 1024 * (1 + (lw >> 1)) + 128 * I.m;
  const unsigned cF = TYPE ? 448 * I.m + 64 * mo + 256 : 128 * I.m, cH = 448 * I.m + (mo ? 0 : 384);
  const unsigned lb = lw * 4096;
  const u4_t dws = mkdesc(P.ws, WS_END);
  auto issue = [&](int u) {
    if (uskip(TYPE, u)) return;
    const unsigned l0 = slot(u) + lb;
    if (u < 16) {
      const unsigned kc = ((u >> 1) + I.rot) & 7;
      if ((u & 1) == 0) {
#pragma unroll
        for (int p = 0; p < 4; ++p) dma(l0 + p * 1024, (p & 1) ? vx1 : vx0, dx, xb + kc * 128 + p * 8192);
      } else {
#pragma unroll
        for (int p = 0; p < 4; ++p) dma(l0 + p * 1024, vw, dw, (kc * 64 + (lw & 1) * 32 + p * 8) * WSTR + c1);
      }
    } else if (u < 22) {
      if (u == 17 || u == 20) {
        const unsigned kc = ((u == 17 ? 0 : 4) + lw + I.rot) & 7;
#pragma unroll
        for (int p = 0; p < 4; ++p) dma(l0 + p * 1024, vh, dw, (kc * 64 + p * 16) * WSTR + cH);
      } else {
        const int n = u == 16 ? 0 : u == 18 ? 1 : u == 19 ? 2 : 3;
        const unsigned kc = (2 * n + (lw >> 1) + I.rot) & 7;
#pragma unroll
        for (int p = 0; p < 4; ++p) dma(l0 + p * 1024, vw, dw, (kc * 64 + (lw & 1) * 32 + p * 8) * WSTR + cF);
      }
    } else {
      const int q = u - 22;
      const unsigned ts = unit_tile(q, I.c) + lw;
      unsigned row;
      if (q < 8) { const unsigned mp = ts >> 2; row = 224 * mp + 32 * (mp & 1) + 32 * lw; }
      else if (q < 14) {
        const unsigned tt = ts - 32, mp = (tt * 43) >> 7, t3 = tt - 3 * mp;
        row = 224 * mp + ((mp & 1) ? (t3 < 2 ? 160 + 32 * t3 : 0) : 128 + 32 * t3);
      } else row = 32 * (ts - 56);
#pragma unroll
      for (int p = 0; p < 4; ++p) dma(l0 + p * 1024, v2, q < 14 ? d2 : dwo, (row + p * 8) * 1024 + I.c * 128);
    }
  };
  STAMP(0);
#pragma unroll
  for (int u = 0; u <= UPRO; ++u) issue(u);
  auto step = [&](auto tc) {
    constexpr int t = decltype(tc)::value;
    vmcnt<vmwait(TYPE, t)>();
    bar();
    STAMP(1 + t);
    if (TYPE == 0 && t == 11) {
      const unsigned fo = WS_KVF + (I.m * 4 + lw) * 128;
      waitflag(dws, (lane & 15) * 4, fo, ld32(ws, (lane & 15) * 4, fo), __builtin_amdgcn_readfirstlane(*(AS3 unsigned*)L_TAG));
      const unsigned pb = WS_KVP + I.m * 65536;
      u2_t v[16];
      unsigned zz[16];
#pragma unroll
      for (int r = 0; r < 16; ++r) v[r] = __builtin_amdgcn_raw_buffer_load_b64(ws, lw * 512 + lane * 8, pb + r * 4096, SC1);
      if (lw == 0) {
#pragma unroll
        for (int r = 0; r < 16; ++r) zz[r] = ld32(ws, 2048 + lane * 4, pb + r * 4096);
      }
      float s0 = 0, s1 = 0, s2 = 0, s3 = 0;
#pragma unroll
      for (int r = 0; r < 16; ++r) { s0 += bflo(v[r][0]); s1 += bfhi(v[r][0]); s2 += bflo(v[r][1]); s3 += bfhi(v[r][1]); }
      *(AS3 u2_t*)(L_KVF + (lw >> 1) * 1024 + lane * 16 + (lw & 1) * 8) = u2_t{pk(s0, s1), pk(s2, s3)};
      if (lw == 0) {
        float z = 0;
#pragma unroll
        for (int r = 0; r < 16; ++r) z += __builtin_bit_cast(float, zz[r]);
        *(AS3 unsigned short*)(L_ZF + lane * 2) = (unsigned short)pk(z, z);
      }
      STAMP(24);
    }
#pragma unroll
    for (int u = uafter(t - 1) + 1; u <= uafter(t); ++u) issue(u);
  };
  sfor<NSTEP>(step);
}

template <int TYPE> DI void compute(const Params& P, const Ids& I, int w, int lane, rsrc_t ws, unsigned& prof) {
  const int tid = w * 64 + lane, h = lane >> 5, mo = I.m & 1;
  STAMP(0);
  const unsigned rtag = __builtin_amdgcn_readfirstlane(ld32(ws, 0, WS_EP + I.i * 128)) + 1;
  const unsigned atag = __builtin_amdgcn_readfirstlane(ld32(ws, 0, WS_EP + 2048)) + 1;
  if (TYPE) {
    if (tid < 224) {
      const int t = tid >> 5, rel = mo ? (t < 6 ? 32 + tid : tid - 192) : tid;
      *(AS3 float*)(L_B1 + tid * 4) = bflo(*(const unsigned short*)(P.b1 + (224 * I.m + rel) * 2));
    }
  } else {
    *(AS3 unsigned*)(L_GATE + tid * 4) = *(const unsigned*)(P.gw + ((tid * 4 + I.rot * 128) & 1023));
  }
  if (tid < 64) *(AS3 float*)(L_B2 + tid * 4) = bflo(*(const unsigned short*)(P.b2 + (64 * I.c + tid) * 2));
  const unsigned r4 = (lane >> 2) & 3, trc = ((lane >> 4) & 1) * 32 + (lane & 3) * 8, tsw = (r4 >> 1) & 1;
  unsigned tA0 = h * 1024 + r4 * 128 + 64 * tsw + trc, tA1 = h * 1024 + r4 * 128 + 64 * (tsw ^ 1) + trc;
  unsigned tH = h * 512 + r4 * 64 + trc + shi(17), mb = MISC + h * 16;
  PIN(mb);
  const unsigned row = lane & 31;
  unsigned xa[4];
#pragma unroll
  for (int s = 0; s < 4; ++s) xa[s] = w * 4096 + row * 128 + ((2 * s + h) ^ ((row >> 1) & 7)) * 16;
  const unsigned ptile = I.i * 4 + w;
  const unsigned pv = ptile * (72 * 2048) + lane * 16;
  const unsigned ctile = I.i * 4 + I.rh * 2 + (w & 1);
  const unsigned cv = ctile * (72 * 2048) + (w >> 1) * 4096 + lane * 16;
  auto bias = [&](unsigned a) {
    f16_t r;
#pragma unroll
    for (int q = 0; q < 4; ++q) { f4_t b = ldsf4(mb + (a - MISC) + q * 32); r[4 * q] = b[0]; r[4 * q + 1] = b[1]; r[4 * q + 2] = b[2]; r[4 * q + 3] = b[3]; }
    return r;
  };
  const f16_t zero = {};
  const u4_t dws = mkdesc(P.ws, WS_END);
  const unsigned l0 = lane ? OOB : 0;
  if (!TYPE) *(AS3 unsigned*)L_TAG = atag;
  asm volatile("" ::"s"(rtag), "s"(atag));
  bar();
  STAMP(1);
  bf8_t xf[32];
  bf4_t W[2][8];
  f16_t acc[4];
  auto a1 = [&](int ks, int r) { return slo(2 * (ks >> 2) + 1) + (r >> 2) * 8192 + (ks & 3) * 2048 + (r & 1) * 512 + ((r & 2) ? tA1 : tA0); };
  xf[0] = lds128(slot(0) + xa[0]);
#pragma unroll
  for (int r = 0; r < 8; ++r) W[0][r] = ldstr(a1(0, r));
#pragma unroll
  for (int t = 0; t < 4; ++t) acc[t] = TYPE ? bias(L_B1 + t * 128) : zero;
  SB();
  sfor<128>([&](auto ic) {
    constexpr int ks = ic.value >> 2, t = ic.value & 3, cb = ks & 1, nb = cb ^ 1, n = ks + 1;
    {
      const bf8_t wf = cat(W[cb][2 * t], W[cb][2 * t + 1]);
      acc[t] = TYPE ? MFMA(wf, xf[ks], acc[t]) : MFMA(xf[ks], wf, acc[t]);
      PIN(acc[t]);
      SB();
      if (t == 0 && (ks & 3) == 3) { bar(); STAMP(2 + (ks >> 2)); }
      if (n < 32) {
        if (t == 0) {
          if (n >= 4) mv<2 * (n >> 2) - 2, 2 * (n >> 2)>(xa[n & 3]);
          if ((n & 3) == 0) { mv<2 * (n >> 2) - 1, 2 * (n >> 2) + 1>(tA0); mv<2 * (n >> 2) - 1, 2 * (n >> 2) + 1>(tA1); }
        }
        if (t == 0) { xf[n & 31] = lds128(slo(2 * (n >> 2)) + xa[n & 3]); W[nb][0] = ldstr(a1(n, 0)); W[nb][1] = ldstr(a1(n, 1)); }
        if (t == 1) { W[nb][2] = ldstr(a1(n, 2)); W[nb][3] = ldstr(a1(n, 3)); W[nb][4] = ldstr(a1(n, 4)); }
        if (t == 2) { W[nb][5] = ldstr(a1(n, 5)); W[nb][6] = ldstr(a1(n, 6)); W[nb][7] = ldstr(a1(n, 7)); }
        SB();
      }
    }
  });
  auto relu2 = [&](f16_t a, unsigned ts) {
#pragma unroll
    for (int r = 0; r < 16; ++r) { const float v = relu(a[r]); a[r] = v * v; }
    __builtin_amdgcn_raw_buffer_store_b128(pk8(a, 0), ws, pv, ts * 2048, SC1);
    __builtin_amdgcn_raw_buffer_store_b128(pk8(a, 1), ws, pv + 1024, ts * 2048, SC1);
  };
  if (TYPE) {
#pragma unroll
    for (int t = 0; t < 4; ++t) relu2(acc[t], 4 * I.m + t);
  } else {
    const bf8_t ones = asbf(u4_t{0x3f803f80u, 0x3f803f80u, 0x3f803f80u, 0x3f803f80u});
#pragma unroll
    for (int T = 0; T < 2; ++T) {
      f16_t k = acc[T];
#pragma unroll
      for (int r = 0; r < 16; ++r) k[r] = phi(k[r]);
      const bf8_t p0 = asbf(pk8(k, 0)), p1 = asbf(pk8(k, 1));
      f16_t kv = MFMA(p0, asbf(pk8(acc[2 + T], 0)), zero), z = MFMA(p0, ones, zero);
      kv = MFMA(p1, asbf(pk8(acc[2 + T], 1)), kv);
      z = MFMA(p1, ones, z);
      const bool hi = lane & 16;
#pragma unroll
      for (int q = 0; q < 2; ++q) {
        f4_t o;
#pragma unroll
        for (int j = 0; j < 4; ++j) { float lo_ = kv[4 * q + j]; PIN(lo_); o[j] = hi ? kv[8 + 4 * q + j] : lo_; }
        *(AS3 f4_t*)(L_KVS + w * 4096 + (2 * T + q) * 1024 + lane * 16) = o;
      }
      {
        const unsigned za = row == 0 ? L_ZS + w * 256 + h * 64 : L_DUMP + tid * 16;
#pragma unroll
        for (int q = 0; q < 4; ++q) *(AS3 f4_t*)(za + T * 128 + q * 16) = f4_t{z[4 * q], z[4 * q + 1], z[4 * q + 2], z[4 * q + 3]};
      }
    }
  }
  STAMP(24);
  auto a2 = [&](int ks, int r) {
    return r < 4 ? slo(ufull(ks)) + (ks & 7) * 2048 + (r & 1) * 512 + ((r & 2) ? tA1 : tA0) : slo(ks < 16 ? 17 : 20) + (ks & 15) * 1024 + (r & 1) * 256 + tH;
  };
  bf8_t G[2];
  mv<15, 16>(tA0); mv<15, 16>(tA1);
  PIN(tH);
#pragma unroll
  for (int r = 0; r < (TYPE ? 6 : 4); ++r) W[0][r] = ldstr(a2(0, r));
  if (!TYPE) G[0] = lds128(mb);
#pragma unroll
  for (int t = 0; t < 3; ++t) acc[t] = TYPE ? bias(L_B1 + (4 + t) * 128) : zero;
  const unsigned fv = (lane & 7) * 4, fs = WS_HF + ctile * 384;
  unsigned fval = 0;
  SB();
  sfor<96>([&](auto ic) {
    constexpr int ks = ic.value / 3, t = ic.value % 3, cb = ks & 1, nb = cb ^ 1, n = ks + 1;
    {
      const bf8_t wf = (TYPE || t < 2) ? cat(W[cb][2 * t], W[cb][2 * t + 1]) : G[cb];
      acc[t] = MFMA(wf, xf[ks], acc[t]);
      PIN(acc[t]);
      SB();
      if (t == 0 && (ks & 7) == 7) { bar(); STAMP(10 + (ks >> 3)); }
      if (n < 32) {
        if (t == 0) { mv<ufull(ks), ufull(n)>(tA0); mv<ufull(ks), ufull(n)>(tA1); if (TYPE && n == 16) mv<17, 20>(tH); }
        if (t == 0) { W[nb][0] = ldstr(a2(n, 0)); W[nb][1] = ldstr(a2(n, 1)); W[nb][2] = ldstr(a2(n, 2)); }
        if (t == 1) {
          W[nb][3] = ldstr(a2(n, 3));
          if (TYPE) { W[nb][4] = ldstr(a2(n, 4)); W[nb][5] = ldstr(a2(n, 5)); } else G[nb] = lds128(mb + n * 32);
        }
      }
      if (t == 2) {
        if (TYPE && ks == 8) { vmcnt<0>(); __builtin_amdgcn_raw_buffer_store_b32(rtag, ws, l0, WS_HF + ptile * 384 + I.m * 4, SC1); }
        if (!TYPE && ks == 8) {
          f4_t s = ldsf4(L_KVS + w * 1024 + lane * 16);
#pragma unroll
          for (int q = 1; q < 4; ++q) s += ldsf4(L_KVS + q * 4096 + w * 1024 + lane * 16);
          const unsigned pb = WS_KVP + I.m * 65536 + I.i * 4096;
          __builtin_amdgcn_raw_buffer_store_b64(u2_t{pk(s[0], s[1]), pk(s[2], s[3])}, ws, tid * 8, pb, SC1);
          {
            float z = 0;
#pragma unroll
            for (int q = 0; q < 4; ++q) z += *(AS3 float*)(L_ZS + q * 256 + lane * 4);
            __builtin_amdgcn_raw_buffer_store_b32(__builtin_bit_cast(unsigned, z), ws, (w ? OOB : 2048) + lane * 4, pb, SC1);
          }
        }
        if (!TYPE && ks == 16) { vmcnt<0>(); __builtin_amdgcn_raw_buffer_store_b32(atag, ws, l0, WS_KVF + (I.m * 4 + w) * 128 + I.i * 4, SC1); }
        if (ks == 25) fval = ld32(ws, fv, fs);
      }
      SB();
    }
  });
  u4_t H[18][4];
  auto hload = [&](int u) {
    const unsigned so = unit_tile(u, I.c) * 2048;
#pragma unroll
    for (int q = 0; q < 4; ++q) H[u][q] = __builtin_amdgcn_raw_buffer_load_b128(ws, cv + q * 1024, so, SC1);
  };
  waitflag(dws, fv, fs, fval, rtag);
  STAMP(25);
#pragma unroll
  for (int u = 0; u < PDIST; ++u) hload(u);
  SB();
  if (TYPE) {
#pragma unroll
    for (int t = 0; t < 3; ++t) relu2(acc[t], 32 + 3 * I.m + t);
  } else {
    const bool hi = lane & 16;
    const u4_t z4 = {};
#pragma unroll
    for (int T = 0; T < 2; ++T) {
      f16_t q = acc[T];
#pragma unroll
      for (int r = 0; r < 16; ++r) q[r] = phi(q[r]);
      const bf8_t p0 = asbf(pk8(q, 0)), p1 = asbf(pk8(q, 1));
      const u4_t kf = *(AS3 u4_t*)(L_KVF + T * 1024 + lane * 16);
      f16_t num = MFMA(asbf(hi ? z4 : kf), p0, zero);
      const f16_t d0 = MFMA(lds128(L_ZF + T * 64 + h * 32), p0, zero);
      num = MFMA(asbf(hi ? kf : z4), p1, num);
      const f16_t d1 = MFMA(lds128(L_ZF + T * 64 + h * 32 + 16), p1, zero);
      const float r0 = __builtin_amdgcn_rcpf(d0[0] + 1e-6f), r1 = __builtin_amdgcn_rcpf(d1[0] + 1e-6f);
#pragma unroll
      for (int r = 0; r < 16; ++r) num[r] *= r < 8 ? r0 : r1;
      __builtin_amdgcn_raw_buffer_store_b128(pk8(num, 0), ws, pv, (56 + 2 * I.m + T) * 2048, SC1);
      __builtin_amdgcn_raw_buffer_store_b128(pk8(num, 1), ws, pv + 1024, (56 + 2 * I.m + T) * 2048, SC1);
    }
    const float g = __builtin_amdgcn_rcpf(1.f + __builtin_amdgcn_exp2f(acc[2][0] * -1.44269504088896f));
    __builtin_amdgcn_raw_buffer_store_b32(__builtin_bit_cast(unsigned, g), ws, lane * 4, WS_G + (ptile * 8 + I.m) * 256, SC1);
  }
  STAMP(26);
  unsigned tB0 = shi(22) + (w >> 1) * 8192 + (h * 4 + r4) * 128 + 64 * tsw + trc, tB1 = tB0 ^ 64;
  PIN(tB0); PIN(tB1);
  auto a3 = [&](int gi, int r) { return slo(22 + (gi >> 1)) + (gi & 1) * 4096 + (r >> 2) * 2048 + (r & 1) * 1024 + ((r & 2) ? tB1 : tB0); };
#pragma unroll
  for (int r = 0; r < 8; ++r) W[0][r] = ldstr(a3(0, r));
  acc[0] = bias(L_B2);
  acc[1] = bias(L_B2 + 128);
#pragma unroll
  for (int r = 0; r < 16; ++r) { acc[0][r] = w >= 2 ? 0.f : acc[0][r]; acc[1][r] = w >= 2 ? 0.f : acc[1][r]; }
  acc[2] = zero;
  acc[3] = zero;
  unsigned gbits = 0;
  SB();
  sfor<144>([&](auto ic) {
    constexpr int gi = ic.value >> 2, t = ic.value & 3, cb = gi & 1, nb = cb ^ 1, n = gi + 1, u = gi >> 1, ab = u < 14 ? 0 : 2;
    {
      acc[ab + (t & 1)] = MFMA(cat(W[cb][2 * t], W[cb][2 * t + 1]), asbf(H[u][(gi & 1) * 2 + (t >> 1)]), acc[ab + (t & 1)]);
      PIN(acc[ab + (t & 1)]);
      SB();
      if (t == 0 && (gi & 3) == 3 && n < 36) { bar(); STAMP(14 + (gi >> 2)); }
      if (n < 36) {
        if (t == 0) { mv<22 + (gi >> 1), 22 + (n >> 1)>(tB0); mv<22 + (gi >> 1), 22 + (n >> 1)>(tB1); }
        if (t == 0) { W[nb][0] = ldstr(a3(n, 0)); W[nb][1] = ldstr(a3(n, 1)); W[nb][2] = ldstr(a3(n, 2)); }
        if (t == 1) { W[nb][3] = ldstr(a3(n, 3)); W[nb][4] = ldstr(a3(n, 4)); W[nb][5] = ldstr(a3(n, 5)); }
        if (t == 2) { W[nb][6] = ldstr(a3(n, 6)); W[nb][7] = ldstr(a3(n, 7)); }
      }
      if (t == 3 && (gi & 1) == 0) {
        constexpr int v = u + PDIST;
        if (gi == 2) { vmcnt<4>(); __builtin_amdgcn_raw_buffer_store_b32(rtag, ws, l0, WS_HF + ptile * 384 + I.m * 4 + (TYPE ? 128 : 256), SC1); }
        if (v == 8 || v == 14) waitflag(dws, fv, fs + (v == 8 ? 128 : 256), fval, rtag);
        if (v == 7 || v == 13) fval = ld32(ws, fv, fs + (v == 7 ? 128 : 256));
        if (v < 18) hload(v < 18 ? v : 0);
        if (v == 18) gbits = ld32(ws, lane * 4, WS_G + (ctile * 8 + I.c) * 256);
      }
      SB();
    }
  });
  STAMP(27);
  const float g = __builtin_bit_cast(float, gbits), g1 = 1.f - g;
#pragma unroll
  for (int T = 0; T < 2; ++T)
#pragma unroll
    for (int q = 0; q < 4; ++q) {
      f4_t o;
#pragma unroll
      for (int j = 0; j < 4; ++j) o[j] = g * acc[2 + T][4 * q + j] + g1 * acc[T][4 * q + j];
      *(AS3 f4_t*)(L_RED + ((w >> 1) * 64 + (w & 1) * 32 + row) * 272 + T * 128 + q * 32 + h * 16) = o;
    }
  if (tid == 0 && I.c == 0 && I.rh == 0) {
    __builtin_amdgcn_raw_buffer_store_b32(rtag, ws, 0, WS_EP + I.i * 128, SC1);
    if (I.i == 0) __builtin_amdgcn_raw_buffer_store_b32(atag, ws, 0, WS_EP + 2048, SC1);
  }
}

__global__ __launch_bounds__(512, 2) void fused_block(Params P) {
  __shared__ __attribute__((aligned(1024))) char lds[163840];
  asm volatile("" ::"v"((AS3 char*)lds) : "memory");
  const int b = blockIdx.x, xcd = b & 7, s = b >> 3, il = s >> 3, jl = s & 7;
  Ids I;
  I.i = (xcd >> 1) * 4 + il; I.m = (xcd & 1) * 4 + (jl >> 1); I.c = jl; I.rh = xcd & 1; I.rot = (jl + 2 * il) & 7;
  const int w = __builtin_amdgcn_readfirstlane(threadIdx.x >> 6), lane = threadIdx.x & 63;
  const rsrc_t ws = __builtin_amdgcn_make_buffer_rsrc(P.ws, 0, WS_END, 0x00020000);
  unsigned prof = 0;
  if (w >= 4) {
    if (jl & 1) loader<1>(P, I, w - 4, lane, ws, prof); else loader<0>(P, I, w - 4, lane, ws, prof);
  } else {
    if (jl & 1) compute<1>(P, I, w, lane, ws, prof); else compute<0>(P, I, w, lane, ws, prof);
  }
  bar();
  const int t = threadIdx.x >> 3, cq = threadIdx.x & 7;
  const unsigned a = L_RED + t * 272 + cq * 32;
  const f4_t v0 = ldsf4(a) + ldsf4(a + 64 * 272), v1 = ldsf4(a + 16) + ldsf4(a + 64 * 272 + 16);
  *(u4_t*)(P.out + (I.i * 128 + I.rh * 64 + t) * 1024 + I.c * 128 + cq * 16) = u4_t{pk(v0[0], v0[1]), pk(v0[2], v0[3]), pk(v1[0], v1[1]), pk(v1[2], v1[3])};
  STAMP(28);
}

extern "C" void kernel_launch(void* const* d_in, const int* in_sizes, int n_in, void* d_out, int out_size, void* d_ws,
                              size_t ws_size, hipStream_t stream) {
  Params P;
  P.x = (const char*)d_in[0]; P.wqkv = (const char*)d_in[1]; P.wo = (const char*)d_in[2]; P.w1 = (const char*)d_in[4];
  P.b1 = (const char*)d_in[5]; P.w2 = (const char*)d_in[6]; P.b2 = (const char*)d_in[7]; P.gw = (const char*)d_in[8];
  P.out = (char*)d_out; P.ws = (char*)d_ws;
  fused_block<<<256, 512, 0, stream>>>(P);
}
```

```cpp
#include <hip/hip_runtime.h>
#include <utility>

typedef __attribute__((ext_vector_type(8))) __bf16 bf8_t;
typedef __attribute__((ext_vector_type(4))) __bf16 bf4_t;
typedef __attribute__((ext_vector_type(2))) __bf16 bf2_t;
typedef __attribute__((ext_vector_type(16))) float f16_t;
typedef __attribute__((ext_vector_type(4))) float f4_t;
typedef __attribute__((ext_vector_type(2))) float f2_t;
typedef __attribute__((ext_vector_type(4))) unsigned u4_t;
typedef __attribute__((ext_vector_type(2))) unsigned u2_t;
typedef __amdgpu_buffer_rsrc_t rsrc_t;
#define AS3 __attribute__((address_space(3)))
#define DI __device__ __forceinline__
#define SB() __builtin_amdgcn_sched_barrier(0)
#define MFMA(a, b, c) __builtin_amdgcn_mfma_f32_32x32x16_bf16((a), (b), (c), 0, 0, 0)
#define SC1 16

constexpr unsigned WS_KVP = 0x900000, WS_G = 0x980000, WS_HF = 0x9A0000, WS_KVF = 0x9A8000, WS_EP = 0x9B0000,
                   WS_XF = 0x9C0000, WS_PROF = 0xA00000, WS_X = 0xB00000, WS_END = 0xF00000;
constexpr unsigned SLOT = 16384, MISC = 9 * SLOT, L_GATE = MISC, L_B1 = MISC + 1024, L_B2 = MISC + 2048,
                   L_ZS = MISC + 2560, L_KVF = MISC + 4096, L_ZF = MISC + 6144, L_TAG = MISC + 6400, L_DUMP = MISC + 8192, L_KVS = 8 * SLOT, L_RED = 6 * SLOT;
constexpr unsigned CODE_BYTES = 40960;
constexpr int SPIN_MAX = 1 << 17;
#define PDIST 8
#define ROTX ((il + (jl >> 1)) & 1)
constexpr int NSTEP = 21;
#define GSTEP 11
#define XGI 32
constexpr int OGI = 4 * (GSTEP + 3 - 12);
constexpr int UPRO = 3;
constexpr int uafter(int t) {
  constexpr int U[NSTEP] = {5, 7, 9, 11, 13, 15, 17, 20, 23, 25, 27, 28, 30, 31, 32, 32, 32, 32, 32, 32, 32};
  return t < 0 ? UPRO : U[t];
}
constexpr int uneed(int t) { return t < 8 ? 2 * t + 1 : t == 8 ? 17 : t == 9 ? 18 : t == 10 ? 20 : t == 11 ? 21 : t < 19 ? 10 + t : 30 + 2 * (t - 19); }
constexpr bool uskip(int type, int u) { return type == 0 && (u == 17 || u == 20); }
constexpr int vmwait(int type, int t) {
  int n = 0;
  for (int u = uneed(t) + 1; u <= uafter(t - 1); ++u) n += uskip(type, u) ? 0 : 4;
  if (type == 0 && t > GSTEP && uneed(t) <= uafter(GSTEP - 1)) n += 1;
  if (type == 0 && t > GSTEP + 1 && uneed(t) <= uafter(GSTEP)) n += 20;
  return n;
}
constexpr unsigned slot(int u) { return (unsigned)(u % 9) * SLOT; }
constexpr unsigned slo(int u) { return slot(u) & 0xffffu; }
constexpr unsigned shi(int u) { return slot(u) & ~0xffffu; }
constexpr int ufull(int ks) { return ks < 8 ? 16 : ks < 16 ? 18 : ks < 24 ? 19 : 21; }

#define RSTAMP(id)
#define STAMP(id)

struct Params {
  const char *x, *wqkv, *wo, *w1, *b1, *w2, *b2, *gw;
  char *out, *ws;
};
struct Ids { int i, m, c, rh, rot, s; unsigned long long pc; };

template <int N> DI void vmcnt() { asm volatile("s_waitcnt vmcnt(%0)" ::"n"(N) : "memory"); }
DI void bar() { asm volatile("s_waitcnt lgkmcnt(0)\n\ts_barrier" ::: "memory"); SB(); }
DI bf8_t lds128(unsigned a) { return *(AS3 bf8_t*)a; }
DI f4_t ldsf4(unsigned a) { return *(AS3 f4_t*)a; }
DI bf4_t ldstr(unsigned a) { return __builtin_amdgcn_ds_read_tr16_b64_v4bf16((AS3 bf4_t*)a); }
DI bf8_t cat(bf4_t a, bf4_t b) { return __builtin_shufflevector(a, b, 0, 1, 2, 3, 4, 5, 6, 7); }
DI unsigned pk(float a, float b) { f2_t f = {a, b}; return __builtin_bit_cast(unsigned, __builtin_convertvector(f, bf2_t)); }
DI float bflo(unsigned v) { return __builtin_bit_cast(float, v << 16); }
DI float bfhi(unsigned v) { return __builtin_bit_cast(float, v & 0xffff0000u); }
DI u4_t pk8(const f16_t& a, int s) { return u4_t{pk(a[8 * s], a[8 * s + 1]), pk(a[8 * s + 2], a[8 * s + 3]), pk(a[8 * s + 4], a[8 * s + 5]), pk(a[8 * s + 6], a[8 * s + 7])}; }
DI bf8_t asbf(u4_t v) { return __builtin_bit_cast(bf8_t, v); }
DI float relu(float t) { return __builtin_bit_cast(float, max(__builtin_bit_cast(int, t), 0)); }
DI float phi(float t) {
  float e = __builtin_amdgcn_fmed3f(__builtin_amdgcn_exp2f(t * 1.44269504088896f), 0.f, 1.f);
  return (relu(t) + e) + 1e-6f;
}
DI u4_t mkdesc(const void* p, unsigned bytes) {
  unsigned long long a = (unsigned long long)p;
  return u4_t{(unsigned)a, (unsigned)(a >> 32) & 0xffffu, bytes, 0x00020000u};
}
DI void dma(unsigned ldsa, unsigned voff, u4_t d, unsigned soff) {
  asm volatile("s_mov_b32 m0, %0\n\ts_nop 0\n\tbuffer_load_dwordx4 %1, %2, %3 offen lds" ::"s"(ldsa), "v"(voff), "s"(d), "s"(soff) : "memory");
}
DI unsigned ld32(rsrc_t r, unsigned v, unsigned s) { return __builtin_amdgcn_raw_buffer_load_b32(r, v, s, SC1); }
DI void waitflag(u4_t d, unsigned v, unsigned s, unsigned val, unsigned tag) {
  unsigned n = SPIN_MAX;
  asm volatile("s_nop 4\n1%=:\n\tv_cmp_ne_u32 vcc, %4, %0\n\ts_cbranch_vccz 2%=f\n\ts_sleep 1\n\tbuffer_load_dword %0, %2, %3, %5 offen sc1\n\t"
               "s_sub_u32 %1, %1, 1\n\ts_waitcnt vmcnt(0)\n\ts_cbranch_scc0 1%=b\n2%=:"
               : "+v"(val), "+s"(n) : "v"(v), "s"(d), "s"(tag), "s"(s) : "memory", "vcc", "scc");
}
template <class F, int... T> DI void sfor_(F&& f, std::integer_sequence<int, T...>) { (f(std::integral_constant<int, T>{}), ...); }
template <int N, class F> DI void sfor(F&& f) { sfor_(f, std::make_integer_sequence<int, N>{}); }
#define PIN(x) asm volatile("" : "+v"(x))
template <int FROM, int TO> DI void mv(unsigned& a) { if (shi(FROM) != shi(TO)) { a += shi(TO) - shi(FROM); PIN(a); } }
constexpr unsigned OOB = 0x80000000u;
DI int unit_tile(int u, int c, int rh) { return u < 4 ? 16 * rh + 4 * ((u + c) & 3) : u < 7 ? 32 + 12 * rh + 4 * ((u - 4 + c) % 3) : 56 + 4 * ((u - 7 + c) & 3); }
constexpr int gunit(int gi) { return gi < 28 ? gi >> 2 : 7 + ((gi - 28) >> 1); }

template <int TYPE> DI void loader(const Params& P, const Ids& I, int lw, int lane, rsrc_t ws, unsigned& prof) {
  constexpr unsigned WSTR = TYPE ? 3584 : 3072;
  const u4_t dx = mkdesc(P.x, 2048 * 1024), dw = TYPE ? mkdesc(P.w1, 512 * 3584) : mkdesc(P.wqkv, 512 * 3072);
  const u4_t d2 = mkdesc(P.w2, 1792 * 1024), dwo = mkdesc(P.wo, 512 * 1024);
  const unsigned r8 = lane >> 3, ch = lane & 7, sw = (ch ^ (((lane >> 4) & 1) << 2)) * 16;
  const unsigned vx0 = r8 * 1024 + (ch ^ (lane >> 4)) * 16, vx1 = r8 * 1024 + (ch ^ (4 + (lane >> 4))) * 16;
  const unsigned vw = r8 * WSTR + sw, vh = (lane >> 2) * WSTR + (lane & 3) * 16, v2 = r8 * 1024 + sw;
  const unsigned mo = I.m & 1;
  const unsigned xb = (I.i * 128 + lw * 32) * 1024;
  const unsigned c1 = TYPE ? 448 * I.m + 64 * mo + 128 * (lw >> 1) : 1024 * (1 + (lw >> 1)) + 128 * I.m;
  const unsigned cF = TYPE ? 448 * I.m + 64 * mo + 256 : 128 * I.m, cH = 448 * I.m + (mo ? 0 : 384);
  const unsigned lb = lw * 4096;
  const u4_t dws = mkdesc(P.ws, WS_END);
  const unsigned kfv = (lane & 15) * 4, kfo = WS_KVF + (I.m * 4 + lw) * 128;
  unsigned kf;
  u2_t gk[16];
  float gz[4];
  auto issue = [&](int u) {
    if (uskip(TYPE, u)) return;
    const unsigned l0 = slot(u) + lb;
    if (u < 16) {
      const unsigned kc = ((u >> 1) + I.rot) & 7;
      if ((u & 1) == 0) {
#pragma unroll
        for (int p = 0; p < 4; ++p) dma(l0 + p * 1024, (p & 1) ? vx1 : vx0, dx, xb + kc * 128 + p * 8192);
      } else {
#pragma unroll
        for (int p = 0; p < 4; ++p) dma(l0 + p * 1024, vw, dw, (kc * 64 + (lw & 1) * 32 + p * 8) * WSTR + c1);
      }
    } else if (u < 22) {
      if (u == 17 || u == 20) {
        const unsigned kc = ((u == 17 ? 0 : 4) + lw + I.rot) & 7;
#pragma unroll
        for (int p = 0; p < 4; ++p) dma(l0 + p * 1024, vh, dw, (kc * 64 + p * 16) * WSTR + cH);
      } else {
        const int n = u == 16 ? 0 : u == 18 ? 1 : u == 19 ? 2 : 3;
        const unsigned kc = (2 * n + (lw >> 1) + I.rot) & 7;
#pragma unroll
        for (int p = 0; p < 4; ++p) dma(l0 + p * 1024, vw, dw, (kc * 64 + (lw & 1) * 32 + p * 8) * WSTR + cF);
      }
    } else {
      const int q = u - 22;
      const unsigned ts = unit_tile(q, I.c, I.rh) + lw;
      unsigned row;
      if (q < 4) { const unsigned mp = ts >> 2; row = 224 * mp + 32 * (mp & 1) + 32 * lw; }
      else if (q < 7) {
        const unsigned tt = ts - 32, mp = (tt * 43) >> 7, t3 = tt - 3 * mp;
        row = 224 * mp + ((mp & 1) ? (t3 < 2 ? 160 + 32 * t3 : 0) : 128 + 32 * t3);
      } else row = 32 * (ts - 56);
#pragma unroll
      for (int p = 0; p < 4; ++p) dma(l0 + p * 1024, v2, q < 7 ? d2 : dwo, (row + p * 8) * 1024 + I.c * 128);
    }
  };
  STAMP(0);
#pragma unroll
  for (int u = 0; u <= UPRO; ++u) issue(u);
  auto step = [&](auto tc) {
    constexpr int t = decltype(tc)::value;
    vmcnt<vmwait(TYPE, t)>();
    bar();
    STAMP(1 + t);
    if (TYPE == 0 && t == GSTEP) asm volatile("buffer_load_dword %0, %1, %2, %3 offen sc1" : "=v"(kf) : "v"(kfv), "s"(dws), "s"(kfo) : "memory");
    if (TYPE == 0 && t == GSTEP + 1) {
      vmcnt<4 * (uafter(GSTEP) - uafter(GSTEP - 1))>();
      waitflag(dws, kfv, kfo, kf, __builtin_amdgcn_readfirstlane(*(AS3 unsigned*)L_TAG));
      STAMP(24);
      const unsigned pb = WS_KVP + I.m * 65536, gv = lw * 512 + lane * 8, zv = 2048 + lw * 64 + (lane >> 4) * 4096 + (lane & 15) * 4;
#pragma unroll
      for (int r = 0; r < 16; ++r) asm volatile("buffer_load_dwordx2 %0, %1, %2, %3 offen sc1" : "=v"(gk[r]) : "v"(gv), "s"(dws), "s"(pb + r * 4096) : "memory");
#pragma unroll
      for (int r = 0; r < 4; ++r) asm volatile("buffer_load_dword %0, %1, %2, %3 offen sc1" : "=v"(gz[r]) : "v"(zv), "s"(dws), "s"(pb + r * 16384) : "memory");
    }
#pragma unroll
    for (int u = uafter(t - 1) + 1; u <= uafter(t); ++u) issue(u);
    if (TYPE == 0 && t == GSTEP + 2) {
      vmcnt<4 * (uafter(GSTEP + 2) - uafter(GSTEP))>();
      float s0 = 0, s1 = 0, s2 = 0, s3 = 0;
#pragma unroll
      for (int r = 0; r < 16; ++r) { PIN(gk[r]); s0 += bflo(gk[r][0]); s1 += bfhi(gk[r][0]); s2 += bflo(gk[r][1]); s3 += bfhi(gk[r][1]); }
      *(AS3 u2_t*)(L_KVF + (lw >> 1) * 1024 + lane * 16 + (lw & 1) * 8) = u2_t{pk(s0, s1), pk(s2, s3)};
#pragma unroll
      for (int r = 0; r < 4; ++r) PIN(gz[r]);
      float z = (gz[0] + gz[1]) + (gz[2] + gz[3]);
      z += __shfl_xor(z, 16);
      z += __shfl_xor(z, 32);
      *(AS3 unsigned short*)(L_ZF + lw * 32 + (lane & 15) * 2) = (unsigned short)pk(z, z);
      STAMP(25);
    }
  };
  sfor<NSTEP>(step);
}

template <int TYPE> DI void compute(const Params& P, const Ids& I, int w, int lane, rsrc_t ws, unsigned& prof) {
  const int tid = w * 64 + lane, h = lane >> 5, mo = I.m & 1;
  STAMP(0);
  unsigned pf;
  asm volatile("global_load_dword %0, %1, %2" : "=v"(pf) : "v"(min(((I.s + 4 * w) & 15) * 4096u + lane * 64u, CODE_BYTES)), "s"(I.pc) : "memory");
  const unsigned e0 = ld32(ws, 0, WS_EP + I.i * 128), e1 = ld32(ws, 0, WS_EP + 2048);
  const unsigned m0 = TYPE ? *(const unsigned short*)(P.b1 + (224 * I.m + (mo ? (tid < 192 ? 32 + tid : tid - 192) : min(tid, 223))) * 2)
                           : *(const unsigned*)(P.gw + ((tid * 4 + I.rot * 128) & 1023));
  const unsigned m1 = *(const unsigned short*)(P.b2 + (64 * I.c + lane) * 2);
  SB();
  const unsigned r4 = (lane >> 2) & 3, trc = ((lane >> 4) & 1) * 32 + (lane & 3) * 8, tsw = (r4 >> 1) & 1;
  unsigned tA0 = h * 1024 + r4 * 128 + 64 * tsw + trc, tA1 = h * 1024 + r4 * 128 + 64 * (tsw ^ 1) + trc;
  unsigned tH = h * 512 + r4 * 64 + trc + shi(17), mb = MISC + h * 16;
  PIN(mb);
  const unsigned row = lane & 31;
  unsigned xa[4];
#pragma unroll
  for (int s = 0; s < 4; ++s) xa[s] = w * 4096 + row * 128 + ((2 * s + h) ^ ((row >> 1) & 7)) * 16;
  const unsigned ptile = I.i * 4 + w;
  const unsigned pv = ptile * (72 * 2048) + lane * 16;
  const unsigned ctile = I.i * 4 + I.rh * 2 + (w & 1);
  const unsigned cv = ctile * (72 * 2048) + (w >> 1) * 4096 + lane * 16;
  auto bias = [&](unsigned a) {
    f16_t r;
#pragma unroll
    for (int q = 0; q < 4; ++q) { f4_t b = ldsf4(mb + (a - MISC) + q * 32); r[4 * q] = b[0]; r[4 * q + 1] = b[1]; r[4 * q + 2] = b[2]; r[4 * q + 3] = b[3]; }
    return r;
  };
  const f16_t zero = {};
  const u4_t dws = mkdesc(P.ws, WS_END);
  const unsigned l0 = lane ? OOB : 0;
  SB();
  *(AS3 unsigned*)((TYPE ? L_B1 : L_GATE) + tid * 4) = TYPE ? m0 << 16 : m0;
  *(AS3 unsigned*)(L_B2 + lane * 4) = m1 << 16;
  const unsigned rtag = __builtin_amdgcn_readfirstlane(e0) + 1, atag = __builtin_amdgcn_readfirstlane(e1) + 1;
  if (!TYPE) *(AS3 unsigned*)L_TAG = atag;
  asm volatile("" ::"s"(rtag), "s"(atag), "v"(pf));
  bar();
  STAMP(1);
  bf8_t xf[32];
  bf4_t W[2][8];
  f16_t acc[4];
  auto a1 = [&](int ks, int r) { return slo(2 * (ks >> 2) + 1) + (r >> 2) * 8192 + (ks & 3) * 2048 + (r & 1) * 512 + ((r & 2) ? tA1 : tA0); };
  xf[0] = lds128(slot(0) + xa[0]);
#pragma unroll
  for (int r = 0; r < 8; ++r) W[0][r] = ldstr(a1(0, r));
#pragma unroll
  for (int t = 0; t < 4; ++t) acc[t] = TYPE ? bias(L_B1 + t * 128) : zero;
  SB();
  sfor<128>([&](auto ic) {
    constexpr int ks = ic.value >> 2, t = ic.value & 3, cb = ks & 1, nb = cb ^ 1, n = ks + 1;
    {
      const bf8_t wf = cat(W[cb][2 * t], W[cb][2 * t + 1]);
      acc[t] = TYPE ? MFMA(wf, xf[ks], acc[t]) : MFMA(xf[ks], wf, acc[t]);
      PIN(acc[t]);
      SB();
      if (t == 0 && (ks & 3) == 3) { bar(); STAMP(2 + (ks >> 2)); }
      if (n < 32) {
        if (t == 0) {
          if (n >= 4) mv<2 * (n >> 2) - 2, 2 * (n >> 2)>(xa[n & 3]);
          if ((n & 3) == 0) { mv<2 * (n >> 2) - 1, 2 * (n >> 2) + 1>(tA0); mv<2 * (n >> 2) - 1, 2 * (n >> 2) + 1>(tA1); }
        }
        if (t == 0) { xf[n & 31] = lds128(slo(2 * (n >> 2)) + xa[n & 3]); W[nb][0] = ldstr(a1(n, 0)); W[nb][1] = ldstr(a1(n, 1)); }
        if (t == 1) { W[nb][2] = ldstr(a1(n, 2)); W[nb][3] = ldstr(a1(n, 3)); W[nb][4] = ldstr(a1(n, 4)); }
        if (t == 2) { W[nb][5] = ldstr(a1(n, 5)); W[nb][6] = ldstr(a1(n, 6)); W[nb][7] = ldstr(a1(n, 7)); }
        SB();
      }
    }
  });
  auto relu2 = [&](f16_t a, unsigned ts) {
#pragma unroll
    for (int r = 0; r < 16; ++r) { const float v = relu(a[r]); a[r] = v * v; }
    __builtin_amdgcn_raw_buffer_store_b128(pk8(a, 0), ws, pv, ts * 2048, SC1);
    __builtin_amdgcn_raw_buffer_store_b128(pk8(a, 1), ws, pv + 1024, ts * 2048, SC1);
  };
  if (TYPE) {
#pragma unroll
    for (int t = 0; t < 4; ++t) relu2(acc[t], 4 * I.m + t);
  } else {
    const bf8_t ones = asbf(u4_t{0x3f803f80u, 0x3f803f80u, 0x3f803f80u, 0x3f803f80u});
#pragma unroll
    for (int T = 0; T < 2; ++T) {
      f16_t k = acc[T];
#pragma unroll
      for (int r = 0; r < 16; ++r) k[r] = phi(k[r]);
      const bf8_t p0 = asbf(pk8(k, 0)), p1 = asbf(pk8(k, 1));
      f16_t kv = MFMA(p0, asbf(pk8(acc[2 + T], 0)), zero), z = MFMA(p0, ones, zero);
      kv = MFMA(p1, asbf(pk8(acc[2 + T], 1)), kv);
      z = MFMA(p1, ones, z);
      const bool hi = lane & 16;
#pragma unroll
      for (int q = 0; q < 2; ++q) {
        f4_t o;
#pragma unroll
        for (int j = 0; j < 4; ++j) { float lo_ = kv[4 * q + j]; PIN(lo_); o[j] = hi ? kv[8 + 4 * q + j] : lo_; }
        *(AS3 f4_t*)(L_KVS + w * 4096 + (2 * T + q) * 1024 + lane * 16) = o;
      }
      {
        const unsigned za = row == 0 ? L_ZS + w * 256 + h * 64 : L_DUMP + tid * 16;
#pragma unroll
        for (int q = 0; q < 4; ++q) *(AS3 f4_t*)(za + T * 128 + q * 16) = f4_t{z[4 * q], z[4 * q + 1], z[4 * q + 2], z[4 * q + 3]};
      }
    }
  }
  STAMP(24);
  auto a2 = [&](int ks, int r) {
    return r < 4 ? slo(ufull(ks)) + (ks & 7) * 2048 + (r & 1) * 512 + ((r & 2) ? tA1 : tA0) : slo(ks < 16 ? 17 : 20) + (ks & 15) * 1024 + (r & 1) * 256 + tH;
  };
  bf8_t G[2];
  mv<15, 16>(tA0); mv<15, 16>(tA1);
  PIN(tH);
#pragma unroll
  for (int r = 0; r < (TYPE ? 6 : 4); ++r) W[0][r] = ldstr(a2(0, r));
  if (!TYPE) G[0] = lds128(mb);
#pragma unroll
  for (int t = 0; t < 3; ++t) acc[t] = TYPE ? bias(L_B1 + (4 + t) * 128) : zero;
  const unsigned fv = (lane & 3) * 4, fs = WS_HF + ptile * 384 + I.rh * 16, fv2 = (lane & 7) * 4, fs2 = WS_HF + ctile * 384 + 256;
  unsigned fval = 0;
  SB();
  sfor<96>([&](auto ic) {
    constexpr int ks = ic.value / 3, t = ic.value % 3, cb = ks & 1, nb = cb ^ 1, n = ks + 1;
    {
      const bf8_t wf = (TYPE || t < 2) ? cat(W[cb][2 * t], W[cb][2 * t + 1]) : G[cb];
      acc[t] = MFMA(wf, xf[ks], acc[t]);
      PIN(acc[t]);
      SB();
      if (t == 0 && (ks & 7) == 7) { bar(); STAMP(10 + (ks >> 3)); }
      if (n < 32) {
        if (t == 0) { mv<ufull(ks), ufull(n)>(tA0); mv<ufull(ks), ufull(n)>(tA1); if (TYPE && n == 16) mv<17, 20>(tH); }
        if (t == 0) { W[nb][0] = ldstr(a2(n, 0)); W[nb][1] = ldstr(a2(n, 1)); W[nb][2] = ldstr(a2(n, 2)); }
        if (t == 1) {
          W[nb][3] = ldstr(a2(n, 3));
          if (TYPE) { W[nb][4] = ldstr(a2(n, 4)); W[nb][5] = ldstr(a2(n, 5)); } else G[nb] = lds128(mb + n * 32);
        }
      }
      if (t == 2) {
        if (TYPE && ks == 8) { vmcnt<0>(); __builtin_amdgcn_raw_buffer_store_b32(rtag, ws, l0, WS_HF + ptile * 384 + I.m * 4, SC1); }
        if (!TYPE && ks == 8) {
          f4_t s = ldsf4(L_KVS + w * 1024 + lane * 16);
#pragma unroll
          for (int q = 1; q < 4; ++q) s += ldsf4(L_KVS + q * 4096 + w * 1024 + lane * 16);
          const unsigned pb = WS_KVP + I.m * 65536 + I.i * 4096;
          __builtin_amdgcn_raw_buffer_store_b64(u2_t{pk(s[0], s[1]), pk(s[2], s[3])}, ws, tid * 8, pb, SC1);
          {
            float z = 0;
#pragma unroll
            for (int q = 0; q < 4; ++q) z += *(AS3 float*)(L_ZS + q * 256 + w * 64 + (lane & 15) * 4);
            __builtin_amdgcn_raw_buffer_store_b32(__builtin_bit_cast(unsigned, z), ws, (lane > 15 ? OOB : 2048) + w * 64 + lane * 4, pb, SC1);
          }
        }
        if (!TYPE && ks == 16) { vmcnt<0>(); __builtin_amdgcn_raw_buffer_store_b32(atag, ws, l0, WS_KVF + (I.m * 4 + w) * 128 + I.i * 4, SC1); }
        if (ks == 25) fval = ld32(ws, fv, fs);
      }
      SB();
    }
  });
  u4_t H[36][2], pq[4];
  auto hload = [&](int gi) {
    const unsigned so = (unit_tile(gunit(gi), I.c, I.rh) + (gi < 28 ? gi & 2 : 0)) * 2048;
#pragma unroll
    for (int q = 0; q < 2; ++q) H[gi][q] = __builtin_amdgcn_raw_buffer_load_b128(ws, (gi < 28 ? pv : cv) + (gi & 1) * 2048 + q * 1024, so, SC1);
  };
  waitflag(dws, fv, fs, fval, rtag);
  STAMP(25);
#pragma unroll
  for (int u = 0; u < PDIST; ++u) hload(u);
  SB();
  if (TYPE) {
#pragma unroll
    for (int t = 0; t < 3; ++t) relu2(acc[t], 32 + 3 * I.m + t);
  } else {
#pragma unroll
    for (int T = 0; T < 2; ++T) {
      f16_t q = acc[T];
#pragma unroll
      for (int r = 0; r < 16; ++r) q[r] = phi(q[r]);
      pq[2 * T] = pk8(q, 0); pq[2 * T + 1] = pk8(q, 1);
      PIN(pq[2 * T]); PIN(pq[2 * T + 1]);
    }
    const float g = __builtin_amdgcn_rcpf(1.f + __builtin_amdgcn_exp2f(acc[2][0] * -1.44269504088896f));
    __builtin_amdgcn_raw_buffer_store_b32(__builtin_bit_cast(unsigned, g), ws, lane * 4, WS_G + (ptile * 8 + I.m) * 256, SC1);
  }
  auto ocomp = [&]() {
    const bool hi = lane & 16;
    const u4_t z4 = {};
#pragma unroll
    for (int T = 0; T < 2; ++T) {
      const bf8_t p0 = asbf(pq[2 * T]), p1 = asbf(pq[2 * T + 1]);
      const u4_t kf = *(AS3 u4_t*)(L_KVF + T * 1024 + lane * 16);
      f16_t num = MFMA(asbf(hi ? z4 : kf), p0, zero);
      const f16_t d0 = MFMA(lds128(L_ZF + T * 64 + h * 32), p0, zero);
      num = MFMA(asbf(hi ? kf : z4), p1, num);
      const f16_t d1 = MFMA(lds128(L_ZF + T * 64 + h * 32 + 16), p1, zero);
      const float r0 = __builtin_amdgcn_rcpf(d0[0] + 1e-6f), r1 = __builtin_amdgcn_rcpf(d1[0] + 1e-6f);
#pragma unroll
      for (int r = 0; r < 16; ++r) num[r] *= r < 8 ? r0 : r1;
      __builtin_amdgcn_raw_buffer_store_b128(pk8(num, 0), ws, pv, (56 + 2 * I.m + T) * 2048, SC1);
      __builtin_amdgcn_raw_buffer_store_b128(pk8(num, 1), ws, pv + 1024, (56 + 2 * I.m + T) * 2048, SC1);
    }
  };
  STAMP(26);
  unsigned tB0 = shi(22) + (h * 4 + r4) * 128 + 64 * tsw + trc, tB1 = tB0 ^ 64;
  PIN(tB0); PIN(tB1);
  auto a3 = [&](int gi, int r) { return slo(22 + gunit(gi)) + (gi & (gi < 28 ? 3 : 1)) * 4096 + (r >> 2) * 2048 + (r & 1) * 1024 + ((r & 2) ? tB1 : tB0); };
#pragma unroll
  for (int r = 0; r < 8; ++r) W[0][r] = ldstr(a3(0, r));
  const bool keep = (w >> 1) == I.rh;
  const unsigned xs = WS_X + (I.i * 8 + I.c) * 32768 + w * 8192, xfs = WS_XF + ((I.i * 8 + I.c) * 4 + w) * 128;
  const unsigned xv = lane * 16 + (keep ? OOB : 0);
  acc[0] = bias(L_B2);
  acc[1] = bias(L_B2 + 128);
#pragma unroll
  for (int r = 0; r < 16; ++r) { acc[0][r] = keep ? acc[0][r] : 0.f; acc[1][r] = keep ? acc[1][r] : 0.f; }
  acc[2] = zero;
  acc[3] = zero;
  unsigned gbits = 0;
  SB();
  sfor<144>([&](auto ic) {
    constexpr int gi = ic.value >> 2, t = ic.value & 3, cb = gi & 1, nb = cb ^ 1, n = gi + 1, ab = gi < 28 ? 0 : 2;
    {
      acc[ab + (t & 1)] = MFMA(cat(W[cb][2 * t], W[cb][2 * t + 1]), asbf(H[gi][t >> 1]), acc[ab + (t & 1)]);
      PIN(acc[ab + (t & 1)]);
      SB();
      if (t == 0 && (gi & 3) == 3 && n < 36) { bar(); STAMP(14 + (gi >> 2)); }
      if (n < 36) {
        if (t == 0) { mv<22 + gunit(gi), 22 + gunit(n)>(tB0); mv<22 + gunit(gi), 22 + gunit(n)>(tB1); }
        if (t == 0 && n == 28) { tB0 += (w >> 1) * 8192; tB1 += (w >> 1) * 8192; PIN(tB0); PIN(tB1); }
        if (t == 0) { W[nb][0] = ldstr(a3(n, 0)); W[nb][1] = ldstr(a3(n, 1)); W[nb][2] = ldstr(a3(n, 2)); }
        if (t == 1) { W[nb][3] = ldstr(a3(n, 3)); W[nb][4] = ldstr(a3(n, 4)); W[nb][5] = ldstr(a3(n, 5)); }
        if (t == 2) { W[nb][6] = ldstr(a3(n, 6)); W[nb][7] = ldstr(a3(n, 7)); }
      }
      if (gi >= 28 && gi < 30) {
        constexpr int T = gi & 1;
        __builtin_amdgcn_raw_buffer_store_b128(__builtin_bit_cast(u4_t, f4_t{acc[T][4 * t], acc[T][4 * t + 1], acc[T][4 * t + 2], acc[T][4 * t + 3]}), ws, xv + t * 1024, xs + T * 4096, SC1);
      }
      if (t == 3) {
        constexpr int v = gi + PDIST;
        if (!TYPE && gi == OGI) { ocomp(); SB(); STAMP(22); }
        if (gi == (TYPE ? 2 : OGI + 3)) { vmcnt<TYPE ? 4 : 6>(); __builtin_amdgcn_raw_buffer_store_b32(rtag, ws, l0, WS_HF + ptile * 384 + I.m * 4 + (TYPE ? 128 : 256), SC1); }
        if (v == 16) waitflag(dws, fv, fs + 128, fval, rtag);
        if (v == 28) waitflag(dws, fv2, fs2, fval, rtag);
        if (v == 14) fval = ld32(ws, fv, fs + 128);
        if (v == 26) fval = ld32(ws, fv2, fs2);
        if (v < 36) hload(v < 36 ? v : 0);
        if (v == 35) gbits = ld32(ws, lane * 4, WS_G + (ctile * 8 + I.c) * 256);
        if (gi == XGI) { vmcnt<0>(); __builtin_amdgcn_raw_buffer_store_b32(rtag, ws, l0 | xv, xfs, SC1); }
      }
      SB();
    }
  });
  STAMP(27);
  const float g = __builtin_bit_cast(float, gbits), g1 = keep ? 1.f - g : 0.f;
  if (keep) {
    waitflag(dws, 0, xfs, rtag + 1, rtag);
    STAMP(23);
#pragma unroll
    for (int q = 0; q < 8; ++q) {
      const f4_t x = __builtin_bit_cast(f4_t, __builtin_amdgcn_raw_buffer_load_b128(ws, lane * 16 + (q & 3) * 1024, xs + (q >> 2) * 4096, SC1));
#pragma unroll
      for (int j = 0; j < 4; ++j) acc[q >> 2][(q & 3) * 4 + j] += x[j];
    }
  } else { acc[0] = zero; acc[1] = zero; }
#pragma unroll
  for (int T = 0; T < 2; ++T)
#pragma unroll
    for (int q = 0; q < 4; ++q) {
      f4_t o;
#pragma unroll
      for (int j = 0; j < 4; ++j) o[j] = g * acc[2 + T][4 * q + j] + g1 * acc[T][4 * q + j];
      *(AS3 f4_t*)(L_RED + ((w >> 1) * 64 + (w & 1) * 32 + row) * 272 + T * 128 + q * 32 + h * 16) = o;
    }
  if (tid == 0 && I.c == 0 && I.rh == 0) {
    __builtin_amdgcn_raw_buffer_store_b32(rtag, ws, 0, WS_EP + I.i * 128, SC1);
    if (I.i == 0) __builtin_amdgcn_raw_buffer_store_b32(atag, ws, 0, WS_EP + 2048, SC1);
  }
}

__global__ __launch_bounds__(512, 2) void fused_block(Params P) {
  __shared__ __attribute__((aligned(1024))) char lds[163840];
  asm volatile("" ::"v"((AS3 char*)lds) : "memory");
  const int b = blockIdx.x, xcd = b & 7, s = b >> 3, il = s >> 3, jl = s & 7;
  Ids I;
  I.i = (xcd >> 1) * 4 + il; I.m = (xcd & 1) * 4 + (jl >> 1); I.c = jl; I.rh = xcd & 1; I.rot = ROTX;
  const int w = __builtin_amdgcn_readfirstlane(threadIdx.x >> 6), lane = threadIdx.x & 63;
  const rsrc_t ws = __builtin_amdgcn_make_buffer_rsrc(P.ws, 0, WS_END, 0x00020000);
  unsigned prof = 0;
  RSTAMP(30);
  unsigned long long pc;
  asm volatile("s_getpc_b64 %0" : "=s"(pc));
  I.pc = pc & ~63ull; I.s = s;
  if (w >= 4) {
    if (jl & 1) loader<1>(P, I, w - 4, lane, ws, prof); else loader<0>(P, I, w - 4, lane, ws, prof);
  } else {
    if (jl & 1) compute<1>(P, I, w, lane, ws, prof); else compute<0>(P, I, w, lane, ws, prof);
  }
  bar();
  const int t = threadIdx.x >> 3, cq = threadIdx.x & 7;
  const unsigned a = L_RED + t * 272 + cq * 32;
  const f4_t v0 = ldsf4(a) + ldsf4(a + 64 * 272), v1 = ldsf4(a + 16) + ldsf4(a + 64 * 272 + 16);
  *(u4_t*)(P.out + (I.i * 128 + I.rh * 64 + t) * 1024 + I.c * 128 + cq * 16) = u4_t{pk(v0[0], v0[1]), pk(v0[2], v0[3]), pk(v1[0], v1[1]), pk(v1[2], v1[3])};
  STAMP(28);
  RSTAMP(31);
}

extern "C" void kernel_launch(void* const* d_in, const int* in_sizes, int n_in, void* d_out, int out_size, void* d_ws,
                              size_t ws_size, hipStream_t stream) {
  Params P;
  P.x = (const char*)d_in[0]; P.wqkv = (const char*)d_in[1]; P.wo = (const char*)d_in[2]; P.w1 = (const char*)d_in[4];
  P.b1 = (const char*)d_in[5]; P.w2 = (const char*)d_in[6]; P.b2 = (const char*)d_in[7]; P.gw = (const char*)d_in[8];
  P.out = (char*)d_out; P.ws = (char*)d_ws;
  fused_block<<<256, 512, 0, stream>>>(P);
}
```

```cpp
#include <hip/hip_runtime.h>
#include <utility>

typedef __attribute__((ext_vector_type(8))) __bf16 bf8_t;
typedef __attribute__((ext_vector_type(4))) __bf16 bf4_t;
typedef __attribute__((ext_vector_type(2))) __bf16 bf2_t;
typedef __attribute__((ext_vector_type(16))) float f16_t;
typedef __attribute__((ext_vector_type(4))) float f4_t;
typedef __attribute__((ext_vector_type(2))) float f2_t;
typedef __attribute__((ext_vector_type(4))) unsigned u4_t;
typedef __attribute__((ext_vector_type(2))) unsigned u2_t;
typedef __amdgpu_buffer_rsrc_t rsrc_t;
#define AS3 __attribute__((address_space(3)))
#define DI __device__ __forceinline__
#define SB() __builtin_amdgcn_sched_barrier(0)
#define MFMA(a, b, c) __builtin_amdgcn_mfma_f32_32x32x16_bf16((a), (b), (c), 0, 0, 0)
#define SC1 16
#define HST SC1
#define HLD SC1

constexpr unsigned WS_KVP = 0x900000, WS_G = 0x980000, WS_HF = 0x9C0000, WS_KVF = 0x9A8000, WS_EP = 0x9B0000,
                   WS_PROF = 0xA00000, WS_X = 0xB00000, WS_END = 0xF00000;
constexpr unsigned SLOT = 16384, MISC = 9 * SLOT, L_GATE = MISC, L_B1 = MISC + 1024, L_B2 = MISC + 2048,
                   L_ZS = MISC + 2560, L_KVF = MISC + 4096, L_ZF = MISC + 6144, L_TAG = MISC + 6400, L_DUMP = MISC + 8192, L_KVS = 8 * SLOT, L_RED = 6 * SLOT;
constexpr unsigned CODE_BYTES = 40960;
constexpr int SPIN_MAX = 1 << 17;
#define PDIST 8
#define ROT2 (il + jl)
#define ROTX ((il + (jl >> 1)) & 1)
constexpr int NSTEP = 21;
#define UT1 {5, 7, 9, 11, 13, 15, 17, 18, 20, 22, 24, 25, 26, 27, 28, 29, 30, 31, 32, 32, 32}
#define UT0 {5, 7, 9, 11, 13, 15, 18, 21, 23, 23, 24, 24, 25, 26, 28, 29, 30, 31, 32, 32, 32}
#define SLP 10
#define PRIO 2
#define LGI 9
#define OFG 23
#define XLG 33
#define EFK 14
#define UPF 16
#define UPL 0
#define UPA 0
#define UPQ 0
#define KP (UPA ? 9 : 10)
#define HSP 1
#define PFT 100
#define PFS 1
#define FRH 384
#define FST 768
#define PSL "8"
#define KT 12
#define KS 13
#define LFG 5
#define OFD 3
#define LPRIO 3
constexpr int OGI = 4 * (KS - 11);
constexpr int UPRO = 3;
constexpr int uafter(int type, int t) {
  constexpr int U1[NSTEP] = UT1;
  constexpr int U0[NSTEP] = UT0;
  return t < 0 ? UPRO : type ? U1[t] : U0[t];
}
constexpr int uneed(int type, int t) {
  return t < 8 ? 2 * t + 1 : t == 8 ? 17 : t == 9 ? 18 : t == 10 ? (type ? 20 : 21) : t == 11 ? 21 : t < 19 ? 10 + t : 30 + 2 * (t - 19);
}
constexpr bool uskip(int type, int u) { return type == 0 && (u == 17 || u == 20); }
constexpr int vmwait(int type, int t) {
  int n = 0;
  for (int u = uneed(type, t) + 1; u <= uafter(type, t - 1); ++u) n += uskip(type, u) ? 0 : 4;
  for (int j = 0; j < 3; ++j) n += PFT + j * PFS < t && uafter(type, PFT + j * PFS) >= uneed(type, t);
  if (type == 0 && KS > KT && t == KS) n += 20;
  return n;
}
constexpr int imax(int a, int b) { return a > b ? a : b; }
constexpr int hissue(int v) { return v < 16 ? v - PDIST : v < 28 ? imax(v - PDIST, LGI + (v - 16) / 2) : imax(v - PDIST, OFG + (v - 28) / 2); }
constexpr int nvm(int g0, int g1) { int n = 0; for (int v = 0; v < 36; ++v) n += hissue(v) >= g0 && hissue(v) < g1 ? 2 : 0; return n; }
constexpr unsigned slot(int u) { return (unsigned)(u % 9) * SLOT; }
constexpr unsigned slo(int u) { return slot(u) & 0xffffu; }
constexpr unsigned shi(int u) { return slot(u) & ~0xffffu; }
constexpr int ufull(int ks) { return ks < 8 ? 16 : ks < 16 ? 18 : ks < 24 ? 19 : 21; }

#define RSTAMP(id)
#define STAMP(id)

struct Params {
  const char *x, *wqkv, *wo, *w1, *b1, *w2, *b2, *gw;
  char *out, *ws;
};
struct Ids { int i, m, c, rh, rot, r2, s; unsigned long long pc; };

template <int N> DI void vmcnt() { asm volatile("s_waitcnt vmcnt(%0)" ::"n"(N) : "memory"); }
DI void bar() { asm volatile("s_waitcnt lgkmcnt(0)\n\ts_barrier" ::: "memory"); SB(); }
DI bf8_t lds128(unsigned a) { return *(AS3 bf8_t*)a; }
DI f4_t ldsf4(unsigned a) { return *(AS3 f4_t*)a; }
DI bf4_t ldstr(unsigned a) { return __builtin_amdgcn_ds_read_tr16_b64_v4bf16((AS3 bf4_t*)a); }
DI bf8_t cat(bf4_t a, bf4_t b) { return __builtin_shufflevector(a, b, 0, 1, 2, 3, 4, 5, 6, 7); }
DI unsigned pk(float a, float b) { f2_t f = {a, b}; return __builtin_bit_cast(unsigned, __builtin_convertvector(f, bf2_t)); }
DI float bflo(unsigned v) { return __builtin_bit_cast(float, v << 16); }
DI float bfhi(unsigned v) { return __builtin_bit_cast(float, v & 0xffff0000u); }
DI float dot2(unsigned v, unsigned k, float c) { asm("v_dot2_f32_bf16 %0, %1, %2, %0" : "+v"(c) : "v"(v), "s"(k)); return c; }
DI u4_t pk8(const f16_t& a, int s) { return u4_t{pk(a[8 * s], a[8 * s + 1]), pk(a[8 * s + 2], a[8 * s + 3]), pk(a[8 * s + 4], a[8 * s + 5]), pk(a[8 * s + 6], a[8 * s + 7])}; }
DI bf8_t asbf(u4_t v) { return __builtin_bit_cast(bf8_t, v); }
DI float relu(float t) { return __builtin_bit_cast(float, max(__builtin_bit_cast(int, t), 0)); }
DI float phi(float t) {
  float e = __builtin_amdgcn_fmed3f(__builtin_amdgcn_exp2f(t * 1.44269504088896f), 0.f, 1.f);
  return (relu(t) + e) + 1e-6f;
}
DI float phi1(float t1) {
  float e = __builtin_amdgcn_fmed3f(__builtin_amdgcn_exp2f(__builtin_fmaf(t1, 1.44269504088896f, -1.44269504088896e-6f)), 0.f, 1.f);
  return __builtin_bit_cast(float, max(__builtin_bit_cast(int, t1), __builtin_bit_cast(int, 1e-6f))) + e;
}
typedef __attribute__((ext_vector_type(2))) short s2_t;
DI unsigned relu2pk(float a, float b) {
  float x = a * __builtin_fabsf(a);
  asm("" : "+v"(x));
  const s2_t p = __builtin_bit_cast(s2_t, pk(x, b * __builtin_fabsf(b)));
  return __builtin_bit_cast(unsigned, __builtin_elementwise_max(p, s2_t{0, 0}));
}
DI u4_t mkdesc(const void* p, unsigned bytes) {
  unsigned long long a = (unsigned long long)p;
  return u4_t{(unsigned)a, (unsigned)(a >> 32) & 0xffffu, bytes, 0x00020000u};
}
DI void dma(unsigned ldsa, unsigned voff, u4_t d, unsigned soff) {
  asm volatile("s_mov_b32 m0, %0\n\ts_nop 0\n\tbuffer_load_dwordx4 %1, %2, %3 offen lds" ::"s"(ldsa), "v"(voff), "s"(d), "s"(soff) : "memory");
}
DI unsigned ld32(rsrc_t r, unsigned v, unsigned s) { return __builtin_amdgcn_raw_buffer_load_b32(r, v, s, SC1); }
DI void waitflag(u4_t d, unsigned v, unsigned s, unsigned val, unsigned tag) {
  unsigned n = SPIN_MAX;
  asm volatile("s_nop 4\n1%=:\n\tv_cmp_ne_u32 vcc, %4, %0\n\ts_cbranch_vccz 2%=f\n\ts_sleep " PSL "\n\tbuffer_load_dword %0, %2, %3, %5 offen sc1\n\t"
               "s_sub_u32 %1, %1, 1\n\ts_waitcnt vmcnt(0)\n\ts_cbranch_scc0 1%=b\n2%=:"
               : "+v"(val), "+s"(n) : "v"(v), "s"(d), "s"(tag), "s"(s) : "memory", "vcc", "scc");
}
template <class F, int... T> DI void sfor_(F&& f, std::integer_sequence<int, T...>) { (f(std::integral_constant<int, T>{}), ...); }
template <int N, class F> DI void sfor(F&& f) { sfor_(f, std::make_integer_sequence<int, N>{}); }
#define PIN(x) asm volatile("" : "+v"(x))
template <int FROM, int TO> DI void mv(unsigned& a) { if (shi(FROM) != shi(TO)) { a += shi(TO) - shi(FROM); PIN(a); } }
constexpr unsigned OOB = 0x80000000u;
DI int unit_tile(int u, int c, int rh) { return u < 4 ? 16 * rh + 4 * ((u + c) & 3) : u < 7 ? 32 + 12 * rh + 4 * ((u - 4 + c) % 3) : 56 + 4 * ((u - 7 + c) & 3); }
constexpr int gunit(int gi) { return gi < 28 ? gi >> 2 : 7 + ((gi - 28) >> 1); }

template <int TYPE> DI void loader(const Params& P, const Ids& I, int lw, int lane, rsrc_t ws, unsigned& prof) {
  __builtin_amdgcn_s_setprio(LPRIO);
  constexpr unsigned WSTR = TYPE ? 3584 : 3072;
  const u4_t dx = mkdesc(P.x, 2048 * 1024), dw = TYPE ? mkdesc(P.w1, 512 * 3584) : mkdesc(P.wqkv, 512 * 3072);
  const u4_t d2 = mkdesc(P.w2, 1792 * 1024), dwo = mkdesc(P.wo, 512 * 1024);
  const unsigned r8 = lane >> 3, ch = lane & 7, sw = (ch ^ (((lane >> 4) & 1) << 2)) * 16;
  const unsigned vx0 = r8 * 1024 + (ch ^ (lane >> 4)) * 16, vx1 = r8 * 1024 + (ch ^ (4 + (lane >> 4))) * 16;
  const unsigned vw = r8 * WSTR + sw, vh = (lane >> 2) * WSTR + (lane & 3) * 16, v2 = r8 * 1024 + sw;
  const unsigned mo = I.m & 1;
  const unsigned xb = (I.i * 128 + lw * 32) * 1024;
  const unsigned c1 = TYPE ? 448 * I.m + 64 * mo + 128 * (lw >> 1) : 1024 * (1 + (lw >> 1)) + 128 * I.m;
  const unsigned cF = TYPE ? 448 * I.m + 64 * mo + 256 : 128 * I.m, cH = 448 * I.m + (mo ? 0 : 384);
  const unsigned lb = lw * 4096;
  const u4_t dws = mkdesc(P.ws, WS_END);
  const unsigned kfv = (lane & 15) * 4, kfo = WS_KVF + (I.m * 4 + lw) * 128;
  unsigned atag, pfv = 0;
  u2_t gk[16];
  float gz[4];
  auto issue = [&](int u) {
    if (uskip(TYPE, u)) return;
    const unsigned l0 = slot(u) + lb;
    if (u < 16) {
      const unsigned kc = ((u >> 1) + I.rot) & 7;
      if ((u & 1) == 0) {
#pragma unroll
        for (int p = 0; p < 4; ++p) dma(l0 + p * 1024, (p & 1) ? vx1 : vx0, dx, xb + kc * 128 + p * 8192);
      } else {
#pragma unroll
        for (int p = 0; p < 4; ++p) dma(l0 + p * 1024, vw, dw, (kc * 64 + (lw & 1) * 32 + p * 8) * WSTR + c1);
      }
    } else if (u < 22) {
      if (u == 17 || u == 20) {
        const unsigned kc = ((u == 17 ? 0 : 4) + lw + I.rot) & 7;
#pragma unroll
        for (int p = 0; p < 4; ++p) dma(l0 + p * 1024, vh, dw, (kc * 64 + p * 16) * WSTR + cH);
      } else {
        const int n = u == 16 ? 0 : u == 18 ? 1 : u == 19 ? 2 : 3;
        const unsigned kc = (2 * n + (lw >> 1) + I.rot) & 7;
#pragma unroll
        for (int p = 0; p < 4; ++p) dma(l0 + p * 1024, vw, dw, (kc * 64 + (lw & 1) * 32 + p * 8) * WSTR + cF);
      }
    } else {
      const int q = u - 22;
      const unsigned ts = unit_tile(q, I.r2, I.rh) + lw;
      unsigned row;
      if (q < 4) { const unsigned mp = ts >> 2; row = 224 * mp + 32 * (mp & 1) + 32 * lw; }
      else if (q < 7) {
        const unsigned tt = ts - 32, mp = (tt * 43) >> 7, t3 = tt - 3 * mp;
        row = 224 * mp + ((mp & 1) ? (t3 < 2 ? 160 + 32 * t3 : 0) : 128 + 32 * t3);
      } else row = 32 * (ts - 56);
#pragma unroll
      for (int p = 0; p < 4; ++p) dma(l0 + p * 1024, v2, q < 7 ? d2 : dwo, (row + p * 8) * 1024 + I.c * 128);
    }
  };
  STAMP(0);
#pragma unroll
  for (int u = 0; u <= UPRO; ++u) issue(u);
  auto step = [&](auto tc) {
    constexpr int t = decltype(tc)::value;
    if (TYPE == 0 && t == 11) return;
    vmcnt<vmwait(TYPE, t)>();
    STAMP(33 + t);
    bar();
    STAMP(1 + t);
    const unsigned pb = WS_KVP + I.m * 65536;
    if (TYPE == 0 && t == KP) {
      f4_t s = ldsf4(L_KVS + lb + lane * 16);
      float z = *(AS3 float*)(L_ZS + lw * 64 + (lane & 15) * 4);
#pragma unroll
      for (int q = 1; q < 4; ++q) { s += ldsf4(L_KVS + lb + q * 1024 + lane * 16); z += *(AS3 float*)(L_ZS + q * 256 + lw * 64 + (lane & 15) * 4); }
      atag = __builtin_amdgcn_readfirstlane(*(AS3 unsigned*)L_TAG);
      __builtin_amdgcn_raw_buffer_store_b64(u2_t{pk(s[0], s[1]), pk(s[2], s[3])}, ws, lw * 512 + lane * 8, pb + I.i * 4096, SC1);
      __builtin_amdgcn_raw_buffer_store_b32(__builtin_bit_cast(unsigned, z), ws, (lane > 15 ? OOB : 2048) + lw * 64 + lane * 4, pb + I.i * 4096, SC1);
      vmcnt<0>();
      __builtin_amdgcn_raw_buffer_store_b32(atag, ws, lane ? OOB : 0, kfo + I.i * 4, SC1);
      STAMP(23);
    }
    if (TYPE && (t == 8 || t == 12)) __builtin_amdgcn_s_sleep(SLP);
#pragma unroll
    for (int u = uafter(TYPE, t - 1) + 1; u <= uafter(TYPE, t); ++u) issue(u);
#pragma unroll
    for (int j = 0; j < 3; ++j)
      if (t == PFT + j * PFS) {
        const unsigned q = I.s * 4 + lw + (j == 1 ? 128 : 0);
        asm volatile("buffer_load_dword %0, %1, %2, %3 offen" : "+v"(pfv) : "v"(lane * 64 + (q < 224 ? 0 : OOB)), "s"(j < 2 ? d2 : dwo), "s"(q * 4096 + (j < 2 ? I.rh * 917504 : 0)) : "memory");
      }
    if (TYPE == 0 && t == KT) {
      waitflag(dws, kfv, kfo, atag + 1, atag);
      STAMP(24);
      const unsigned gv = lw * 512 + lane * 8, zv = 2048 + lw * 64 + (lane >> 4) * 4096 + (lane & 15) * 4;
#pragma unroll
      for (int r = 0; r < 16; ++r) asm volatile("buffer_load_dwordx2 %0, %1, %2, %3 offen sc1" : "=v"(gk[r]) : "v"(gv), "s"(dws), "s"(pb + r * 4096) : "memory");
#pragma unroll
      for (int r = 0; r < 4; ++r) asm volatile("buffer_load_dword %0, %1, %2, %3 offen sc1" : "=v"(gz[r]) : "v"(zv), "s"(dws), "s"(pb + r * 16384) : "memory");
    }
    if (TYPE == 0 && t == KS) {
      vmcnt<KS == KT ? 0 : 4 * (uafter(0, KS) - uafter(0, KS - 1))>();
      float s0 = 0, s1 = 0, s2 = 0, s3 = 0;
#pragma unroll
      for (int r = 0; r < 16; ++r) { PIN(gk[r]); s0 = dot2(gk[r][0], 0x3f80u, s0); s1 = dot2(gk[r][0], 0x3f800000u, s1); s2 = dot2(gk[r][1], 0x3f80u, s2); s3 = dot2(gk[r][1], 0x3f800000u, s3); }
      asm volatile("s_nop 4" : "+v"(s0), "+v"(s1), "+v"(s2), "+v"(s3));
      *(AS3 u2_t*)(L_KVF + (lw >> 1) * 1024 + lane * 16 + (lw & 1) * 8) = u2_t{pk(s0, s1), pk(s2, s3)};
#pragma unroll
      for (int r = 0; r < 4; ++r) PIN(gz[r]);
      float z = (gz[0] + gz[1]) + (gz[2] + gz[3]);
      z += __shfl_xor(z, 16);
      z += __shfl_xor(z, 32);
      *(AS3 unsigned short*)(L_ZF + lw * 32 + (lane & 15) * 2) = (unsigned short)pk(z, z);
      STAMP(25);
    }
  };
  sfor<NSTEP>(step);
  asm volatile("" ::"v"(pfv));
}

template <int TYPE> DI void compute(const Params& P, const Ids& I, int w, int lane, rsrc_t ws, unsigned& prof) {
  __builtin_amdgcn_s_setprio(PRIO);
  const int tid = w * 64 + lane, h = lane >> 5, mo = I.m & 1;
  STAMP(0);
  unsigned pf;
  asm volatile("global_load_dword %0, %1, %2" : "=v"(pf) : "v"(min(((I.s + 4 * w) & 15) * 4096u + lane * 64u, CODE_BYTES)), "s"(I.pc) : "memory");
  const unsigned e0 = ld32(ws, 0, WS_EP + I.i * 128), e1 = ld32(ws, 0, WS_EP + 2048);
  const unsigned m0 = TYPE ? *(const unsigned short*)(P.b1 + (224 * I.m + (mo ? (tid < 192 ? 32 + tid : tid - 192) : min(tid, 223))) * 2)
                           : *(const unsigned*)(P.gw + ((tid * 4 + I.rot * 128) & 1023));
  const unsigned m1 = *(const unsigned short*)(P.b2 + (64 * I.c + lane) * 2);
  SB();
  const unsigned r4 = (lane >> 2) & 3, trc = ((lane >> 4) & 1) * 32 + (lane & 3) * 8, tsw = (r4 >> 1) & 1;
  unsigned tA0 = h * 1024 + r4 * 128 + 64 * tsw + trc, tA1 = h * 1024 + r4 * 128 + 64 * (tsw ^ 1) + trc;
  unsigned tH = h * 512 + r4 * 64 + trc + shi(17), mb = MISC + h * 16;
  PIN(mb);
  const unsigned row = lane & 31;
  unsigned xa[4];
#pragma unroll
  for (int s = 0; s < 4; ++s) xa[s] = w * 4096 + row * 128 + ((2 * s + h) ^ ((row >> 1) & 7)) * 16;
  const unsigned ptile = I.i * 4 + w;
  const unsigned pv = ptile * (72 * 2048) + lane * 16;
  const unsigned ctile = I.i * 4 + I.rh * 2 + (w & 1);
  const unsigned cv = ctile * (72 * 2048) + (w >> 1) * 4096 + lane * 16;
  auto bias = [&](unsigned a) {
    f16_t r;
#pragma unroll
    for (int q = 0; q < 4; ++q) { f4_t b = ldsf4(mb + (a - MISC) + q * 32); r[4 * q] = b[0]; r[4 * q + 1] = b[1]; r[4 * q + 2] = b[2]; r[4 * q + 3] = b[3]; }
    return r;
  };
  const f16_t zero = {};
  const f16_t eps = zero + 1e-6f;
  const u4_t dws = mkdesc(P.ws, WS_END);
  const unsigned l0 = lane ? OOB : 0;
  SB();
  *(AS3 unsigned*)((TYPE ? L_B1 : L_GATE) + tid * 4) = TYPE ? m0 << 16 : m0;
  *(AS3 unsigned*)(L_B2 + lane * 4) = m1 << 16;
  const unsigned rtag = __builtin_amdgcn_readfirstlane(e0) + 1, atag = __builtin_amdgcn_readfirstlane(e1) + 1;
  if (!TYPE) { *(AS3 unsigned*)L_TAG = atag; *(AS3 unsigned*)(L_B1 + tid * 4) = 0; }
  asm volatile("" ::"s"(rtag), "s"(atag), "v"(pf));
  bar();
  STAMP(1);
  bf8_t xf[32];
  bf4_t W[2][8];
  f16_t acc[4];
  auto a1 = [&](int ks, int r) { return slo(2 * (ks >> 2) + 1) + (r >> 2) * 8192 + (ks & 3) * 2048 + (r & 1) * 512 + ((r & 2) ? tA1 : tA0); };
  xf[0] = lds128(slot(0) + xa[0]);
#pragma unroll
  for (int r = 0; r < 8; ++r) W[0][r] = ldstr(a1(0, r));
#pragma unroll
  for (int t = 0; t < 4; ++t) acc[t] = TYPE ? bias(L_B1 + t * 128) : t < 2 ? eps : zero;
  SB();
  sfor<128>([&](auto ic) {
    constexpr int ks = ic.value >> 2, t = ic.value & 3, cb = ks & 1, nb = cb ^ 1, n = ks + 1;
    {
      const bf8_t wf = cat(W[cb][2 * t], W[cb][2 * t + 1]);
      acc[t] = TYPE ? MFMA(wf, xf[ks], acc[t]) : MFMA(xf[ks], wf, acc[t]);
      PIN(acc[t]);
      SB();
      if (t == 0 && (ks & 3) == 3) { bar(); STAMP(2 + (ks >> 2)); }
      if (n < 32) {
        if (t == 0) {
          if (n >= 4) mv<2 * (n >> 2) - 2, 2 * (n >> 2)>(xa[n & 3]);
          if ((n & 3) == 0) { mv<2 * (n >> 2) - 1, 2 * (n >> 2) + 1>(tA0); mv<2 * (n >> 2) - 1, 2 * (n >> 2) + 1>(tA1); }
        }
        if (t == 0) { xf[n & 31] = lds128(slo(2 * (n >> 2)) + xa[n & 3]); W[nb][0] = ldstr(a1(n, 0)); W[nb][1] = ldstr(a1(n, 1)); }
        if (t == 1) { W[nb][2] = ldstr(a1(n, 2)); W[nb][3] = ldstr(a1(n, 3)); W[nb][4] = ldstr(a1(n, 4)); }
        if (t == 2) { W[nb][5] = ldstr(a1(n, 5)); W[nb][6] = ldstr(a1(n, 6)); W[nb][7] = ldstr(a1(n, 7)); }
        SB();
      }
    }
  });
  f16_t old[4];
  u4_t E[8];
#pragma unroll
  for (int t = 0; t < 4; ++t) old[t] = acc[t];
  auto hpair = [&](int p, unsigned ts0) {
    unsigned v = relu2pk(old[p >> 3][2 * (p & 7)], old[p >> 3][2 * (p & 7) + 1]);
    PIN(v);
    E[p >> 2][p & 3] = v;
    if ((p & 3) == 3) __builtin_amdgcn_raw_buffer_store_b128(E[p >> 2], ws, pv + ((p >> 2) & 1) * 1024, (ts0 + (p >> 3)) * 2048, HST);
  };
  auto ppair = [&](int p) {
    float x = phi1(old[p >> 3][2 * (p & 7)]);
    asm("" : "+v"(x));
    unsigned v = pk(x, phi1(old[p >> 3][2 * (p & 7) + 1]));
    PIN(v);
    E[p >> 2][p & 3] = v;
  };
  const bool hi = lane & 16;
  f16_t kv, kz;
  auto kvmm = [&](int T) {
    const bf8_t ones = asbf(u4_t{0x3f803f80u, 0x3f803f80u, 0x3f803f80u, 0x3f803f80u});
    kv = MFMA(asbf(E[2 * T]), asbf(E[4 + 2 * T]), zero);
    kz = MFMA(asbf(E[2 * T]), ones, zero);
    kv = MFMA(asbf(E[2 * T + 1]), asbf(E[5 + 2 * T]), kv);
    kz = MFMA(asbf(E[2 * T + 1]), ones, kz);
    PIN(kv); PIN(kz);
  };
  auto kvst = [&](int T, int q) {
    f4_t o;
#pragma unroll
    for (int j = 0; j < 4; ++j) { float lo_ = kv[4 * q + j]; PIN(lo_); o[j] = hi ? kv[8 + 4 * q + j] : lo_; }
    *(AS3 f4_t*)(L_KVS + (2 * T + q) * 4096 + tid * 16) = o;
    const unsigned za = row == 0 ? L_ZS + w * 256 + h * 64 : L_DUMP + tid * 16;
#pragma unroll
    for (int j = 2 * q; j < 2 * q + 2; ++j) *(AS3 f4_t*)(za + T * 128 + j * 16) = f4_t{kz[4 * j], kz[4 * j + 1], kz[4 * j + 2], kz[4 * j + 3]};
  };
  if (TYPE) {
#pragma unroll
    for (int p = 0; p < UPF; ++p) hpair(p, 4 * I.m);
  } else {
#pragma unroll
    for (int q = 0; q < 4; ++q) { E[4 + q] = pk8(old[2 + (q >> 1)], q & 1); PIN(E[4 + q]); }
    if (UPA) {
#pragma unroll
      for (int p = 0; p < 8; ++p) ppair(p);
      kvmm(0);
#pragma unroll
      for (int p = 8; p < 16; ++p) ppair(p);
      kvst(0, 0); kvst(0, 1);
      kvmm(1);
      kvst(1, 0); kvst(1, 1);
    }
  }
  STAMP(24);
  auto a2 = [&](int ks, int r) {
    return r < 4 ? slo(ufull(ks)) + (ks & 7) * 2048 + (r & 1) * 512 + ((r & 2) ? tA1 : tA0) : slo(ks < 16 ? 17 : 20) + (ks & 15) * 1024 + (r & 1) * 256 + tH;
  };
  bf8_t G[2];
  mv<15, 16>(tA0); mv<15, 16>(tA1);
  PIN(tH);
#pragma unroll
  for (int r = 0; r < (TYPE ? 6 : 4); ++r) W[0][r] = ldstr(a2(0, r));
  unsigned gb = mb + ((((lane >> 4) ^ lane) & 1) ? L_B1 - L_GATE : 0);
  PIN(gb);
  f4_t ga = {};
  if (!TYPE) G[0] = lds128(gb);
#pragma unroll
  for (int t = 0; t < 3; ++t) acc[t] = TYPE ? bias(L_B1 + (4 + t) * 128) : eps;
  const unsigned fv = (lane & 3) * 4, fs = WS_HF + ptile * FST + I.rh * (16 + FRH), fv2 = (lane & 7) * 4, fs2 = WS_HF + ctile * FST + 256;
  unsigned fval = 0;
  u4_t H[36][2];
  auto hload = [&](auto gc) {
    constexpr int gi = gc.value;
    const unsigned so = (unit_tile(gunit(gi), I.r2, I.rh) + (gi < 28 ? gi & 2 : 0)) * 2048;
#pragma unroll
    for (int q = 0; q < 2; ++q) H[gi][q] = __builtin_amdgcn_raw_buffer_load_b128(ws, (gi < 28 ? pv : cv) + (gi & 1) * 2048 + q * 1024, so, gi < 28 ? HLD : SC1);
  };
  SB();
  sfor<96>([&](auto ic) {
    constexpr int ks = ic.value / 3, t = ic.value % 3, cb = ks & 1, nb = cb ^ 1, n = ks + 1;
    {
      if (TYPE || t < 2) { acc[t] = MFMA(cat(W[cb][2 * t], W[cb][2 * t + 1]), xf[ks], acc[t]); PIN(acc[t]); }
      else { ga = __builtin_amdgcn_mfma_f32_16x16x32_bf16(G[cb], xf[ks], ga, 0, 0, 0); PIN(ga); }
      SB();
      if (t == 0 && (TYPE ? (ks & 7) == 7 : (ks == (UPA ? 0 : 7) || ks == 11 || ks == 31))) { bar(); STAMP(10 + (ks >> 3)); }
      if (n < 32) {
        if (t == 0) { mv<ufull(ks), ufull(n)>(tA0); mv<ufull(ks), ufull(n)>(tA1); if (TYPE && n == 16) mv<17, 20>(tH); }
        if (t == 0) { W[nb][0] = ldstr(a2(n, 0)); W[nb][1] = ldstr(a2(n, 1)); W[nb][2] = ldstr(a2(n, 2)); }
        if (t == 1) {
          W[nb][3] = ldstr(a2(n, 3));
          if (TYPE) { W[nb][4] = ldstr(a2(n, 4)); W[nb][5] = ldstr(a2(n, 5)); } else G[nb] = lds128(gb + n * 32);
        }
      }
      if (TYPE) {
        if (ic.value % HSP == 0 && ic.value / HSP < 32 - UPF) hpair((UPF + ic.value / HSP) & 31, 4 * I.m);
        if (t == 2 && ks == EFK) { vmcnt<0>(); __builtin_amdgcn_raw_buffer_store_b32(rtag, ws, l0, WS_HF + ptile * FST + I.m * 4 + I.rh * FRH, SC1); STAMP(29); }
      } else if (!UPA && ks < 11) {
        if (t < 2 && ks < 4) ppair((2 * ks + t) & 15);
        if (t < 2 && ks >= 5 && ks < 9) ppair((2 * ks + t - 2) & 15);
        if (t == 0 && (ks == 4 || ks == 9)) kvmm(ks == 9);
        if (t == 2 && (ks == 4 || ks == 9)) kvst(ks == 9, 0);
        if (t == 2 && ks == 5) kvst(0, 1);
        if (t == 0 && ks == 10) kvst(1, 1);
      }
      if (t == 2) {
        if (ks == 27 - PDIST) fval = ld32(ws, fv, fs);
        if (ks == 32 - PDIST) { waitflag(dws, fv, fs, fval, rtag); STAMP(25); }
        if (ks >= 32 - PDIST) hload(std::integral_constant<int, (ks + PDIST) & 31>{});
      }
      SB();
    }
  });
#pragma unroll
  for (int t = 0; t < 3; ++t) old[t] = acc[t];
  auto ocomp = [&]() {
    const u4_t z4 = {};
#pragma unroll
    for (int T = 0; T < 2; ++T) {
      const bf8_t p0 = asbf(E[2 * T]), p1 = asbf(E[2 * T + 1]);
      const u4_t kf = *(AS3 u4_t*)(L_KVF + T * 1024 + lane * 16);
      f16_t num = MFMA(asbf(hi ? z4 : kf), p0, zero);
      const f16_t d0 = MFMA(lds128(L_ZF + T * 64 + h * 32), p0, zero);
      num = MFMA(asbf(hi ? kf : z4), p1, num);
      const f16_t d1 = MFMA(lds128(L_ZF + T * 64 + h * 32 + 16), p1, zero);
      const float r0 = __builtin_amdgcn_rcpf(d0[0] + 1e-6f), r1 = __builtin_amdgcn_rcpf(d1[0] + 1e-6f);
#pragma unroll
      for (int r = 0; r < 16; ++r) num[r] *= r < 8 ? r0 : r1;
      __builtin_amdgcn_raw_buffer_store_b128(pk8(num, 0), ws, pv, (56 + 2 * I.m + T) * 2048, SC1);
      __builtin_amdgcn_raw_buffer_store_b128(pk8(num, 1), ws, pv + 1024, (56 + 2 * I.m + T) * 2048, SC1);
    }
  };
  if (TYPE && UPL) {
#pragma unroll
    for (int p = 0; p < 24; ++p) hpair(p, 32 + 3 * I.m);
  }
  if (!TYPE && UPQ) {
#pragma unroll
    for (int p = 0; p < 16; ++p) ppair(p);
  }
  STAMP(26);
  unsigned tB0 = shi(22) + (h * 4 + r4) * 128 + 64 * tsw + trc, tB1 = tB0 ^ 64;
  PIN(tB0); PIN(tB1);
  auto a3 = [&](int gi, int r) { return slo(22 + gunit(gi)) + (gi & (gi < 28 ? 3 : 1)) * 4096 + (r >> 2) * 2048 + (r & 1) * 1024 + ((r & 2) ? tB1 : tB0); };
#pragma unroll
  for (int r = 0; r < 8; ++r) W[0][r] = ldstr(a3(0, r));
  const bool keep = (w >> 1) == I.rh;
  const unsigned xs = WS_X + (I.i * 8 + I.c) * 32768 + w * 8192;
  const unsigned xv = lane * 16 + (keep ? OOB : 0);
  acc[0] = bias(L_B2);
  acc[1] = bias(L_B2 + 128);
#pragma unroll
  for (int r = 0; r < 16; ++r) { acc[0][r] = keep ? acc[0][r] : 0.f; acc[1][r] = keep ? acc[1][r] : 0.f; }
  acc[2] = zero;
  acc[3] = zero;
  unsigned gbits = 0;
  u4_t X[8];
  SB();
  sfor<144>([&](auto ic) {
    constexpr int gi = ic.value >> 2, t = ic.value & 3, cb = gi & 1, nb = cb ^ 1, n = gi + 1, ab = gi < 28 ? 0 : 2;
    {
      acc[ab + (t & 1)] = MFMA(cat(W[cb][2 * t], W[cb][2 * t + 1]), asbf(H[gi][t >> 1]), acc[ab + (t & 1)]);
      PIN(acc[ab + (t & 1)]);
      SB();
      if (t == 0 && (gi & 3) == 3 && n < 36) { bar(); STAMP(14 + (gi >> 2)); }
      if (n < 36) {
        if (t == 0) { mv<22 + gunit(gi), 22 + gunit(n)>(tB0); mv<22 + gunit(gi), 22 + gunit(n)>(tB1); }
        if (t == 0 && n == 28) { tB0 += (w >> 1) * 8192; tB1 += (w >> 1) * 8192; PIN(tB0); PIN(tB1); }
        if (t == 0) { W[nb][0] = ldstr(a3(n, 0)); W[nb][1] = ldstr(a3(n, 1)); W[nb][2] = ldstr(a3(n, 2)); }
        if (t == 1) { W[nb][3] = ldstr(a3(n, 3)); W[nb][4] = ldstr(a3(n, 4)); W[nb][5] = ldstr(a3(n, 5)); }
        if (t == 2) { W[nb][6] = ldstr(a3(n, 6)); W[nb][7] = ldstr(a3(n, 7)); }
      }
      if (TYPE && !UPL && ic.value < 16) {
        constexpr int p0 = (ic.value & 15) * 3 / 2, p1 = ((ic.value & 15) + 1) * 3 / 2;
        hpair(p0, 32 + 3 * I.m);
        if (p1 - p0 == 2) hpair(p0 + 1, 32 + 3 * I.m);
      }
      if (!TYPE && !UPQ && ic.value < 16) ppair(ic.value & 15);
      if (!TYPE && ic.value == 16) {
        const float g = __builtin_amdgcn_rcpf(1.f + __builtin_amdgcn_exp2f((hi ? ga[1] : ga[0]) * -1.44269504088896f));
        __builtin_amdgcn_raw_buffer_store_b32(__builtin_bit_cast(unsigned, g), ws, lane * 4, WS_G + (ptile * 8 + I.m) * 256, SC1);
      }
      if (gi >= 28 && gi < 30) {
        constexpr int T = gi & 1;
        __builtin_amdgcn_raw_buffer_store_b128(u4_t{pk(acc[T][4 * t], acc[T][4 * t + 1]), rtag, pk(acc[T][4 * t + 2], acc[T][4 * t + 3]), rtag}, ws, xv + t * 1024, xs + T * 4096, SC1);
      }
      if (gi == XLG || gi == XLG + 1) X[(gi - XLG) * 4 + t] = __builtin_amdgcn_raw_buffer_load_b128(ws, (xv ^ OOB) + t * 1024, xs + (gi - XLG) * 4096, SC1);
      if (t == 3) {
        if (!TYPE && gi == OGI) { ocomp(); SB(); STAMP(22); }
        if (gi == (TYPE ? LFG : OGI + OFD)) { vmcnt<TYPE ? nvm(UPL ? 0 : 3, LFG) : nvm(OGI, OGI + OFD)>(); __builtin_amdgcn_raw_buffer_store_b32(rtag, ws, l0, WS_HF + ptile * FST + I.m * 4 + (TYPE ? 128 + I.rh * FRH : 256), SC1); }
        if (gi == hissue(16)) waitflag(dws, fv, fs + 128, fval, rtag);
        if (gi == hissue(28)) waitflag(dws, fv2, fs2, fval, rtag);
        if (gi == hissue(16) - 3) fval = ld32(ws, fv, fs + 128);
        if (gi == hissue(28) - 3) fval = ld32(ws, fv2, fs2);
        sfor<36>([&](auto vc) { if (hissue(vc.value) == gi) hload(vc); });
        if (gi == hissue(35)) gbits = ld32(ws, lane * 4, WS_G + (ctile * 8 + I.c) * 256);
      }
      SB();
    }
  });
  STAMP(27);
  const float g = __builtin_bit_cast(float, gbits), g1 = keep ? 1.f - g : 0.f;
  if (keep) {
    for (int n = 0; n < SPIN_MAX; ++n) {
      unsigned bad = 0;
#pragma unroll
      for (int q = 0; q < 8; ++q) bad |= (X[q][1] ^ rtag) | (X[q][3] ^ rtag);
      if (__builtin_amdgcn_ballot_w64(bad != 0) == 0) break;
#pragma unroll
      for (int q = 0; q < 8; ++q) X[q] = __builtin_amdgcn_raw_buffer_load_b128(ws, lane * 16 + (q & 3) * 1024, xs + (q >> 2) * 4096, SC1);
      asm volatile("" ::: "memory");
    }
    STAMP(23);
#pragma unroll
    for (int q = 0; q < 8; ++q) {
      acc[q >> 2][(q & 3) * 4] += bflo(X[q][0]); acc[q >> 2][(q & 3) * 4 + 1] += bfhi(X[q][0]);
      acc[q >> 2][(q & 3) * 4 + 2] += bflo(X[q][2]); acc[q >> 2][(q & 3) * 4 + 3] += bfhi(X[q][2]);
    }
  } else { acc[0] = zero; acc[1] = zero; }
#pragma unroll
  for (int T = 0; T < 2; ++T)
#pragma unroll
    for (int q = 0; q < 4; ++q) {
      f4_t o;
#pragma unroll
      for (int j = 0; j < 4; ++j) o[j] = g * acc[2 + T][4 * q + j] + g1 * acc[T][4 * q + j];
      *(AS3 f4_t*)(L_RED + ((w >> 1) * 64 + (w & 1) * 32 + row) * 272 + T * 128 + q * 32 + h * 16) = o;
    }
  if (tid == 0 && I.c == 0 && I.rh == 0) {
    __builtin_amdgcn_raw_buffer_store_b32(rtag, ws, 0, WS_EP + I.i * 128, SC1);
    if (I.i == 0) __builtin_amdgcn_raw_buffer_store_b32(atag, ws, 0, WS_EP + 2048, SC1);
  }
}

__global__ __launch_bounds__(512, 2) void fused_block(Params P) {
  __shared__ __attribute__((aligned(1024))) char lds[163840];
  asm volatile("" ::"v"((AS3 char*)lds) : "memory");
  const int b = blockIdx.x, xcd = b & 7, s = b >> 3, il = s >> 3, jl = s & 7;
  Ids I;
  I.i = (xcd >> 1) * 4 + il; I.m = (xcd & 1) * 4 + (jl >> 1); I.c = jl; I.rh = xcd & 1; I.rot = ROTX; I.r2 = ROT2;
  const int w = __builtin_amdgcn_readfirstlane(threadIdx.x >> 6), lane = threadIdx.x & 63;
  const rsrc_t ws = __builtin_amdgcn_make_buffer_rsrc(P.ws, 0, WS_END, 0x00020000);
  unsigned prof = 0;
  RSTAMP(30);
  unsigned long long pc;
  asm volatile("s_getpc_b64 %0" : "=s"(pc));
  I.pc = pc & ~63ull; I.s = s;
  if (w >= 4) {
    if (jl & 1) loader<1>(P, I, w - 4, lane, ws, prof); else loader<0>(P, I, w - 4, lane, ws, prof);
  } else {
    if (jl & 1) compute<1>(P, I, w, lane, ws, prof); else compute<0>(P, I, w, lane, ws, prof);
  }
  bar();
  const int t = threadIdx.x >> 3, cq = threadIdx.x & 7;
  const unsigned a = L_RED + t * 272 + cq * 32;
  const f4_t v0 = ldsf4(a) + ldsf4(a + 64 * 272), v1 = ldsf4(a + 16) + ldsf4(a + 64 * 272 + 16);
  *(u4_t*)(P.out + (I.i * 128 + I.rh * 64 + t) * 1024 + I.c * 128 + cq * 16) = u4_t{pk(v0[0], v0[1]), pk(v0[2], v0[3]), pk(v1[0], v1[1]), pk(v1[2], v1[3])};
  STAMP(28);
  RSTAMP(31);
}

extern "C" void kernel_launch(void* const* d_in, const int* in_sizes, int n_in, void* d_out, int out_size, void* d_ws,
                              size_t ws_size, hipStream_t stream) {
  Params P;
  P.x = (const char*)d_in[0]; P.wqkv = (const char*)d_in[1]; P.wo = (const char*)d_in[2]; P.w1 = (const char*)d_in[4];
  P.b1 = (const char*)d_in[5]; P.w2 = (const char*)d_in[6]; P.b2 = (const char*)d_in[7]; P.gw = (const char*)d_in[8];
  P.out = (char*)d_out; P.ws = (char*)d_ws;
  fused_block<<<256, 512, 0, stream>>>(P);
}
```

```cpp
#include <hip/hip_runtime.h>
#include <utility>

typedef __attribute__((ext_vector_type(8))) __bf16 bf8_t;
typedef __attribute__((ext_vector_type(4))) __bf16 bf4_t;
typedef __attribute__((ext_vector_type(2))) __bf16 bf2_t;
typedef __attribute__((ext_vector_type(16))) float f16_t;
typedef __attribute__((ext_vector_type(4))) float f4_t;
typedef __attribute__((ext_vector_type(2))) float f2_t;
typedef __attribute__((ext_vector_type(4))) unsigned u4_t;
typedef __attribute__((ext_vector_type(2))) unsigned u2_t;
typedef __amdgpu_buffer_rsrc_t rsrc_t;
#define AS3 __attribute__((address_space(3)))
#define DI __device__ __forceinline__
#define SB() __builtin_amdgcn_sched_barrier(0)
#define MFMA(a, b, c) __builtin_amdgcn_mfma_f32_32x32x16_bf16((a), (b), (c), 0, 0, 0)
#define SC1 16
#define HST SC1
#define HLD SC1

constexpr unsigned WS_KVP = 0x900000, WS_G = 0x980000, WS_HF = 0x9C0000, WS_KVF = 0x9A8000, WS_EP = 0x9B0000,
                   WS_PROF = 0xA00000, WS_X = 0xB00000, WS_END = 0xF00000;
constexpr unsigned SLOT = 16384, MISC = 9 * SLOT, L_GATE = MISC, L_B1 = MISC + 1024, L_B2 = MISC + 2048,
                   L_ZS = MISC + 2560, L_KVF = MISC + 4096, L_ZF = MISC + 6144, L_TAG = MISC + 6400, L_DUMP = MISC + 8192, L_KVS = 8 * SLOT, L_RED = 6 * SLOT;
constexpr unsigned CODE_BYTES = 40960;
constexpr int SPIN_MAX = 1 << 17;
#define PDIST 8
#define ROT2 (il + jl)
#define ROTX ((il + (jl >> 1)) & 1)
constexpr int NSTEP = 21;
#define UT1 {5, 7, 9, 11, 13, 15, 17, 18, 20, 22, 24, 25, 26, 27, 28, 29, 30, 31, 32, 32, 32}
#define UT0 {5, 7, 9, 11, 13, 15, 18, 21, 23, 23, 24, 24, 25, 26, 28, 29, 30, 31, 32, 32, 32}
#define SLP 10
#define PRIO 2
#define LGI 9
#define OFG 23
#define XLG 35
#define EFK 14
#define UPF 16
#define UPL 0
#define UPA 1
#define UPQ 1
#define KP (UPA ? 9 : 10)
#define HSP 1
#define PFT 100
#define PFS 1
#define FRH 384
#define FST 768
#define PSL "4"
#define SL0 0
#define XSL 8
#define HB -1
#define OLD SC1
#define KFK 4
#define TT 100
#define KT 10
#define KS 10
#define LFG 5
#define OFD 3
#define LPRIO 3
constexpr bool OUP = KS == 10;
constexpr int OGI = OUP ? 0 : 4 * (KS - 11);
constexpr int UPRO = 3;
constexpr int uafter(int type, int t) {
  constexpr int U1[NSTEP] = UT1;
  constexpr int U0[NSTEP] = UT0;
  return t < 0 ? UPRO : type ? U1[t] : U0[t];
}
constexpr int uneed(int type, int t) {
  return t < 8 ? 2 * t + 1 : t == 8 ? 17 : t == 9 ? 18 : t == 10 ? (type ? 20 : 21) : t == 11 ? 21 : t < 19 ? 10 + t : 30 + 2 * (t - 19);
}
constexpr bool uskip(int type, int u) { return type == 0 && (u == 17 || u == 20); }
constexpr int vmwait(int type, int t) {
  int n = 0;
  for (int u = uneed(type, t) + 1; u <= uafter(type, t - 1); ++u) n += uskip(type, u) ? 0 : 4;
  for (int j = 0; j < 3; ++j) n += PFT + j * PFS < t && uafter(type, PFT + j * PFS) >= uneed(type, t);
  n += TT < t && uafter(type, TT) >= uneed(type, t);
  if (type == 0 && KS > KT && t == KS) n += 12;
  return n;
}
constexpr int imax(int a, int b) { return a > b ? a : b; }
constexpr int hissue(int v) { return v < 16 ? v - PDIST : v < 28 ? imax(v - PDIST, LGI + (v - 16) / 2) : imax(v - PDIST, OFG + (v - 28) / 2); }
constexpr int nvm(int g0, int g1) { int n = 0; for (int v = 0; v < 36; ++v) n += hissue(v) >= g0 && hissue(v) < g1 ? 2 : 0; return n; }
constexpr unsigned slot(int u) { return (unsigned)(u % 9) * SLOT; }
constexpr unsigned slo(int u) { return slot(u) & 0xffffu; }
constexpr unsigned shi(int u) { return slot(u) & ~0xffffu; }
constexpr int ufull(int ks) { return ks < 8 ? 16 : ks < 16 ? 18 : ks < 24 ? 19 : 21; }

#define RSTAMP(id)
#define STAMP(id)

struct Params {
  const char *x, *wqkv, *wo, *w1, *b1, *w2, *b2, *gw;
  char *out, *ws;
};
struct Ids { int i, m, c, rh, rot, r2, s; unsigned long long pc; };
DI unsigned xaddr(const Ids& I, unsigned v) { asm volatile("" : "+v"(v)); return WS_X + (I.i * 8 + I.c) * 32768 + (2 * I.rh + (v >> 8)) * 8192 + (v & 7) * 1024 + ((v >> 3) & 31) * 16; }

template <int N> DI void vmcnt() { asm volatile("s_waitcnt vmcnt(%0)" ::"n"(N) : "memory"); }
DI void bar() { asm volatile("s_waitcnt lgkmcnt(0)\n\ts_barrier" ::: "memory"); SB(); }
DI bf8_t lds128(unsigned a) { return *(AS3 bf8_t*)a; }
DI f4_t ldsf4(unsigned a) { return *(AS3 f4_t*)a; }
DI bf4_t ldstr(unsigned a) { return __builtin_amdgcn_ds_read_tr16_b64_v4bf16((AS3 bf4_t*)a); }
DI bf8_t cat(bf4_t a, bf4_t b) { return __builtin_shufflevector(a, b, 0, 1, 2, 3, 4, 5, 6, 7); }
DI unsigned pk(float a, float b) { f2_t f = {a, b}; return __builtin_bit_cast(unsigned, __builtin_convertvector(f, bf2_t)); }
DI float bflo(unsigned v) { return __builtin_bit_cast(float, v << 16); }
DI float bfhi(unsigned v) { return __builtin_bit_cast(float, v & 0xffff0000u); }
DI u4_t pk8(const f16_t& a, int s) { return u4_t{pk(a[8 * s], a[8 * s + 1]), pk(a[8 * s + 2], a[8 * s + 3]), pk(a[8 * s + 4], a[8 * s + 5]), pk(a[8 * s + 6], a[8 * s + 7])}; }
DI bf8_t asbf(u4_t v) { return __builtin_bit_cast(bf8_t, v); }
DI float relu(float t) { return __builtin_bit_cast(float, max(__builtin_bit_cast(int, t), 0)); }
DI float phi(float t) {
  float e = __builtin_amdgcn_fmed3f(__builtin_amdgcn_exp2f(t * 1.44269504088896f), 0.f, 1.f);
  return (relu(t) + e) + 1e-6f;
}
DI float phi1(float t1) {
  float e = __builtin_amdgcn_fmed3f(__builtin_amdgcn_exp2f(__builtin_fmaf(t1, 1.44269504088896f, -1.44269504088896e-6f)), 0.f, 1.f);
  return __builtin_bit_cast(float, max(__builtin_bit_cast(int, t1), __builtin_bit_cast(int, 1e-6f))) + e;
}
typedef __attribute__((ext_vector_type(2))) short s2_t;
DI unsigned relu2pk(float a, float b) {
  float x = a * __builtin_fabsf(a);
  asm("" : "+v"(x));
  const s2_t p = __builtin_bit_cast(s2_t, pk(x, b * __builtin_fabsf(b)));
  return __builtin_bit_cast(unsigned, __builtin_elementwise_max(p, s2_t{0, 0}));
}
DI u4_t mkdesc(const void* p, unsigned bytes) {
  unsigned long long a = (unsigned long long)p;
  return u4_t{(unsigned)a, (unsigned)(a >> 32) & 0xffffu, bytes, 0x00020000u};
}
DI void dma(unsigned ldsa, unsigned voff, u4_t d, unsigned soff) {
  asm volatile("s_mov_b32 m0, %0\n\ts_nop 0\n\tbuffer_load_dwordx4 %1, %2, %3 offen lds" ::"s"(ldsa), "v"(voff), "s"(d), "s"(soff) : "memory");
}
DI unsigned ld32(rsrc_t r, unsigned v, unsigned s) { return __builtin_amdgcn_raw_buffer_load_b32(r, v, s, SC1); }
DI void waitflag(u4_t d, unsigned v, unsigned s, unsigned val, unsigned tag) {
  unsigned n = SPIN_MAX;
  asm volatile("s_nop 4\n1%=:\n\tv_cmp_ne_u32 vcc, %4, %0\n\ts_cbranch_vccz 2%=f\n\ts_sleep " PSL "\n\tbuffer_load_dword %0, %2, %3, %5 offen sc1\n\t"
               "s_sub_u32 %1, %1, 1\n\ts_waitcnt vmcnt(0)\n\ts_cbranch_scc0 1%=b\n2%=:"
               : "+v"(val), "+s"(n) : "v"(v), "s"(d), "s"(tag), "s"(s) : "memory", "vcc", "scc");
}
template <class F, int... T> DI void sfor_(F&& f, std::integer_sequence<int, T...>) { (f(std::integral_constant<int, T>{}), ...); }
template <int N, class F> DI void sfor(F&& f) { sfor_(f, std::make_integer_sequence<int, N>{}); }
#define PIN(x) asm volatile("" : "+v"(x))
template <int FROM, int TO> DI void mv(unsigned& a) { if (shi(FROM) != shi(TO)) { a += shi(TO) - shi(FROM); PIN(a); } }
constexpr unsigned OOB = 0x80000000u;
DI int unit_tile(int u, int c, int rh) { return u < 4 ? 16 * rh + 4 * ((u + c) & 3) : u < 7 ? 32 + 12 * rh + 4 * ((u - 4 + c) % 3) : 56 + 4 * ((u - 7 + c) & 3); }
constexpr int gunit(int gi) { return gi < 28 ? gi >> 2 : 7 + ((gi - 28) >> 1); }

template <int TYPE> DI void loader(const Params& P, const Ids& I, int lw, int lane, rsrc_t ws, u4_t* X, unsigned& prof) {
  __builtin_amdgcn_s_setprio(LPRIO);
  constexpr unsigned WSTR = TYPE ? 3584 : 3072;
  const u4_t dx = mkdesc(P.x, 2048 * 1024), dw = TYPE ? mkdesc(P.w1, 512 * 3584) : mkdesc(P.wqkv, 512 * 3072);
  const u4_t d2 = mkdesc(P.w2, 1792 * 1024), dwo = mkdesc(P.wo, 512 * 1024);
  const unsigned r8 = lane >> 3, ch = lane & 7, sw = (ch ^ (((lane >> 4) & 1) << 2)) * 16;
  const unsigned vx0 = r8 * 1024 + (ch ^ (lane >> 4)) * 16, vx1 = r8 * 1024 + (ch ^ (4 + (lane >> 4))) * 16;
  const unsigned vw = r8 * WSTR + sw, vh = (lane >> 2) * WSTR + (lane & 3) * 16, v2 = r8 * 1024 + sw;
  const unsigned mo = I.m & 1;
  const unsigned xb = (I.i * 128 + lw * 32) * 1024;
  const unsigned c1 = TYPE ? 448 * I.m + 64 * mo + 128 * (lw >> 1) : 1024 * (1 + (lw >> 1)) + 128 * I.m;
  const unsigned cF = TYPE ? 448 * I.m + 64 * mo + 256 : 128 * I.m, cH = 448 * I.m + (mo ? 0 : 384);
  const unsigned lb = lw * 4096;
  const u4_t dws = mkdesc(P.ws, WS_END);
  const unsigned kfv = (lane & 15) * 4, kfo = WS_KVF + (I.m * 4 + lw) * 128;
  unsigned atag, pfv = 0;
  u4_t gk[8];
  float gz[4];
  auto issue = [&](int u) {
    if (uskip(TYPE, u)) return;
    const unsigned l0 = slot(u) + lb;
    if (u < 16) {
      const unsigned kc = ((u >> 1) + I.rot) & 7;
      if ((u & 1) == 0) {
#pragma unroll
        for (int p = 0; p < 4; ++p) dma(l0 + p * 1024, (p & 1) ? vx1 : vx0, dx, xb + kc * 128 + p * 8192);
      } else {
#pragma unroll
        for (int p = 0; p < 4; ++p) dma(l0 + p * 1024, vw, dw, (kc * 64 + (lw & 1) * 32 + p * 8) * WSTR + c1);
      }
    } else if (u < 22) {
      if (u == 17 || u == 20) {
        const unsigned kc = ((u == 17 ? 0 : 4) + lw + I.rot) & 7;
#pragma unroll
        for (int p = 0; p < 4; ++p) dma(l0 + p * 1024, vh, dw, (kc * 64 + p * 16) * WSTR + cH);
      } else {
        const int n = u == 16 ? 0 : u == 18 ? 1 : u == 19 ? 2 : 3;
        const unsigned kc = (2 * n + (lw >> 1) + I.rot) & 7;
#pragma unroll
        for (int p = 0; p < 4; ++p) dma(l0 + p * 1024, vw, dw, (kc * 64 + (lw & 1) * 32 + p * 8) * WSTR + cF);
      }
    } else {
      const int q = u - 22;
      const unsigned ts = unit_tile(q, I.r2, I.rh) + lw;
      unsigned row;
      if (q < 4) { const unsigned mp = ts >> 2; row = 224 * mp + 32 * (mp & 1) + 32 * lw; }
      else if (q < 7) {
        const unsigned tt = ts - 32, mp = (tt * 43) >> 7, t3 = tt - 3 * mp;
        row = 224 * mp + ((mp & 1) ? (t3 < 2 ? 160 + 32 * t3 : 0) : 128 + 32 * t3);
      } else row = 32 * (ts - 56);
#pragma unroll
      for (int p = 0; p < 4; ++p) dma(l0 + p * 1024, v2, q < 7 ? d2 : dwo, (row + p * 8) * 1024 + I.c * 128);
    }
  };
  STAMP(0);
#pragma unroll
  for (int u = 0; u <= UPRO; ++u) { if (u == 2 && SL0) __builtin_amdgcn_s_sleep(SL0); issue(u); }
  auto step = [&](auto tc) {
    constexpr int t = decltype(tc)::value;
    if (TYPE == 0 && t == 11) return;
    vmcnt<vmwait(TYPE, t)>();
    STAMP(33 + t);
    bar();
    STAMP(1 + t);
    const unsigned pb = WS_KVP + I.m * 65536;
    if (TYPE == 0 && t == 0) atag = __builtin_amdgcn_readfirstlane(*(AS3 unsigned*)L_TAG);
    if (TYPE == 0 && t == KT) {
      waitflag(dws, kfv, kfo, atag + 1, atag);
      STAMP(24);
      const unsigned gv = lw * 512 + (lane & 31) * 16 + (lane >> 5) * 32768, zv = 2048 + lw * 64 + (lane >> 4) * 4096 + (lane & 15) * 4;
#pragma unroll
      for (int r = 0; r < 8; ++r) asm volatile("buffer_load_dwordx4 %0, %1, %2, %3 offen sc1" : "=v"(gk[r]) : "v"(gv), "s"(dws), "s"(pb + r * 4096) : "memory");
#pragma unroll
      for (int r = 0; r < 4; ++r) asm volatile("buffer_load_dword %0, %1, %2, %3 offen sc1" : "=v"(gz[r]) : "v"(zv), "s"(dws), "s"(pb + r * 16384) : "memory");
    }
    if (TYPE && (t == 8 || t == 12)) __builtin_amdgcn_s_sleep(SLP);
#pragma unroll
    for (int u = uafter(TYPE, t - 1) + 1; u <= uafter(TYPE, t); ++u) issue(u);
    if (t == TT) {
      const char* p = lw == 0 ? P.ws : lw == 1 ? P.out : lw == 2 ? P.w2 : P.wo;
      const unsigned st = lw == 0 ? WS_END / 64 : lw == 1 ? 32768 : lw == 2 ? 28672 : 8192;
      asm volatile("global_load_dword %0, %1, %2 sc1" : "+v"(pfv) : "v"(lane * st), "s"(p) : "memory");
    }
#pragma unroll
    for (int j = 0; j < 3; ++j)
      if (t == PFT + j * PFS) {
        const unsigned q = I.s * 4 + lw + (j == 1 ? 128 : 0);
        asm volatile("buffer_load_dword %0, %1, %2, %3 offen" : "+v"(pfv) : "v"(lane * 64 + (q < 224 ? 0 : OOB)), "s"(j < 2 ? d2 : dwo), "s"(q * 4096 + (j < 2 ? I.rh * 917504 : 0)) : "memory");
      }
    if (TYPE == 0 && t == KS) {
      vmcnt<4 * (uafter(0, KS) - uafter(0, KT - 1))>();
      STAMP(26);
      float sm[8] = {};
#pragma unroll
      for (int r = 0; r < 8; ++r) {
        PIN(gk[r]);
#pragma unroll
        for (int k = 0; k < 8; ++k) sm[k] += k & 1 ? bfhi(gk[r][k >> 1]) : bflo(gk[r][k >> 1]);
      }
#pragma unroll
      for (int k = 0; k < 4; ++k) {
        asm volatile("s_nop 4\n\tv_permlane32_swap_b32 %0, %1\n\ts_nop 1" : "+v"(sm[k]), "+v"(sm[4 + k]));
        sm[k] += sm[4 + k];
      }
      *(AS3 u2_t*)(L_KVF + (lw >> 1) * 1024 + (2 * (lane & 31) + (lane >> 5)) * 16 + (lw & 1) * 8) = u2_t{pk(sm[0], sm[1]), pk(sm[2], sm[3])};
#pragma unroll
      for (int r = 0; r < 4; ++r) PIN(gz[r]);
      float z = (gz[0] + gz[1]) + (gz[2] + gz[3]);
      z += __shfl_xor(z, 16);
      z += __shfl_xor(z, 32);
      *(AS3 unsigned short*)(L_ZF + lw * 32 + (lane & 15) * 2) = (unsigned short)pk(z, z);
      STAMP(25);
    }
  };
  sfor<NSTEP>(step);
  __builtin_amdgcn_s_sleep(XSL);
  const unsigned xa_ = xaddr(I, lw * 64 + 256 + lane);
  X[0] = __builtin_amdgcn_raw_buffer_load_b128(ws, xa_, 0, SC1);
  X[1] = __builtin_amdgcn_raw_buffer_load_b128(ws, xa_ + 512, 0, SC1);
  asm volatile("" ::"v"(pfv));
}

template <int TYPE> DI void compute(const Params& P, const Ids& I, int w, int lane, rsrc_t ws, u4_t* X, unsigned& prof) {
  __builtin_amdgcn_s_setprio(PRIO);
  const int tid = w * 64 + lane, h = lane >> 5, mo = I.m & 1;
  STAMP(0);
  unsigned pf;
  asm volatile("global_load_dword %0, %1, %2" : "=v"(pf) : "v"(min(((I.s + 4 * w) & 15) * 4096u + lane * 64u, CODE_BYTES)), "s"(I.pc) : "memory");
  const unsigned e0 = ld32(ws, 0, WS_EP + I.i * 128), e1 = ld32(ws, 0, WS_EP + 2048);
  const unsigned m0 = TYPE ? *(const unsigned short*)(P.b1 + (224 * I.m + (mo ? (tid < 192 ? 32 + tid : tid - 192) : min(tid, 223))) * 2)
                           : *(const unsigned*)(P.gw + ((tid * 4 + I.rot * 128) & 1023));
  const unsigned m1 = *(const unsigned short*)(P.b2 + (64 * I.c + lane) * 2);
  SB();
  const unsigned r4 = (lane >> 2) & 3, trc = ((lane >> 4) & 1) * 32 + (lane & 3) * 8, tsw = (r4 >> 1) & 1;
  unsigned tA0 = h * 1024 + r4 * 128 + 64 * tsw + trc, tA1 = h * 1024 + r4 * 128 + 64 * (tsw ^ 1) + trc;
  unsigned tH = h * 512 + r4 * 64 + trc + shi(17), mb = MISC + h * 16;
  PIN(mb);
  const unsigned row = lane & 31;
  unsigned xa[4];
#pragma unroll
  for (int s = 0; s < 4; ++s) xa[s] = w * 4096 + row * 128 + ((2 * s + h) ^ ((row >> 1) & 7)) * 16;
  const unsigned ptile = I.i * 4 + w;
  const unsigned pv = ptile * (72 * 2048) + lane * 16;
  const unsigned ctile = I.i * 4 + I.rh * 2 + (w & 1);
  const unsigned cv = ctile * (72 * 2048) + (w >> 1) * 4096 + lane * 16;
  auto bias = [&](unsigned a) {
    f16_t r;
#pragma unroll
    for (int q = 0; q < 4; ++q) { f4_t b = ldsf4(mb + (a - MISC) + q * 32); r[4 * q] = b[0]; r[4 * q + 1] = b[1]; r[4 * q + 2] = b[2]; r[4 * q + 3] = b[3]; }
    return r;
  };
  const f16_t zero = {};
  const f16_t eps = zero + 1e-6f;
  const u4_t dws = mkdesc(P.ws, WS_END);
  const unsigned l0 = lane ? OOB : 0;
  SB();
  *(AS3 unsigned*)((TYPE ? L_B1 : L_GATE) + tid * 4) = TYPE ? m0 << 16 : m0;
  *(AS3 unsigned*)(L_B2 + lane * 4) = m1 << 16;
  const unsigned rtag = __builtin_amdgcn_readfirstlane(e0) + 1, atag = __builtin_amdgcn_readfirstlane(e1) + 1;
  if (!TYPE) { *(AS3 unsigned*)L_TAG = atag; *(AS3 unsigned*)(L_B1 + tid * 4) = 0; }
  asm volatile("" ::"s"(rtag), "s"(atag), "v"(pf));
  bar();
  STAMP(1);
  bf8_t xf[32];
  bf4_t W[2][8];
  f16_t acc[4];
  auto a1 = [&](int ks, int r) { return slo(2 * (ks >> 2) + 1) + (r >> 2) * 8192 + (ks & 3) * 2048 + (r & 1) * 512 + ((r & 2) ? tA1 : tA0); };
  xf[0] = lds128(slot(0) + xa[0]);
#pragma unroll
  for (int r = 0; r < 8; ++r) W[0][r] = ldstr(a1(0, r));
#pragma unroll
  for (int t = 0; t < 4; ++t) acc[t] = TYPE ? bias(L_B1 + t * 128) : t < 2 ? eps : zero;
  SB();
  sfor<128>([&](auto ic) {
    constexpr int ks = ic.value >> 2, t = ic.value & 3, cb = ks & 1, nb = cb ^ 1, n = ks + 1;
    {
      const bf8_t wf = cat(W[cb][2 * t], W[cb][2 * t + 1]);
      acc[t] = TYPE ? MFMA(wf, xf[ks], acc[t]) : MFMA(xf[ks], wf, acc[t]);
      PIN(acc[t]);
      SB();
      if (t == 0 && (ks & 3) == 3) { bar(); STAMP(2 + (ks >> 2)); }
      if (n < 32) {
        if (t == 0) {
          if (n >= 4) mv<2 * (n >> 2) - 2, 2 * (n >> 2)>(xa[n & 3]);
          if ((n & 3) == 0) { mv<2 * (n >> 2) - 1, 2 * (n >> 2) + 1>(tA0); mv<2 * (n >> 2) - 1, 2 * (n >> 2) + 1>(tA1); }
        }
        if (t == 0) { xf[n & 31] = lds128(slo(2 * (n >> 2)) + xa[n & 3]); W[nb][0] = ldstr(a1(n, 0)); W[nb][1] = ldstr(a1(n, 1)); }
        if (t == 1) { W[nb][2] = ldstr(a1(n, 2)); W[nb][3] = ldstr(a1(n, 3)); W[nb][4] = ldstr(a1(n, 4)); }
        if (t == 2) { W[nb][5] = ldstr(a1(n, 5)); W[nb][6] = ldstr(a1(n, 6)); W[nb][7] = ldstr(a1(n, 7)); }
        SB();
      }
    }
  });
  f16_t old[4];
  u4_t E[8];
#pragma unroll
  for (int t = 0; t < 4; ++t) old[t] = acc[t];
  auto hpair = [&](int p, unsigned ts0) {
    unsigned v = relu2pk(old[p >> 3][2 * (p & 7)], old[p >> 3][2 * (p & 7) + 1]);
    PIN(v);
    E[p >> 2][p & 3] = v;
    if ((p & 3) == 3) __builtin_amdgcn_raw_buffer_store_b128(E[p >> 2], ws, pv + ((p >> 2) & 1) * 1024, (ts0 + (p >> 3)) * 2048, HST);
  };
  auto ppair = [&](int p) {
    float x = phi1(old[p >> 3][2 * (p & 7)]);
    asm("" : "+v"(x));
    unsigned v = pk(x, phi1(old[p >> 3][2 * (p & 7) + 1]));
    PIN(v);
    E[p >> 2][p & 3] = v;
  };
  const bool hi = lane & 16;
  f16_t kv, kz;
  auto kvmm = [&](int T) {
    const bf8_t ones = asbf(u4_t{0x3f803f80u, 0x3f803f80u, 0x3f803f80u, 0x3f803f80u});
    kv = MFMA(asbf(E[2 * T]), asbf(E[4 + 2 * T]), zero);
    kz = MFMA(asbf(E[2 * T]), ones, zero);
    kv = MFMA(asbf(E[2 * T + 1]), asbf(E[5 + 2 * T]), kv);
    kz = MFMA(asbf(E[2 * T + 1]), ones, kz);
    PIN(kv); PIN(kz);
  };
  auto kvst = [&](int T, int q) {
    f4_t o;
#pragma unroll
    for (int j = 0; j < 4; ++j) { float lo_ = kv[4 * q + j]; PIN(lo_); o[j] = hi ? kv[8 + 4 * q + j] : lo_; }
    *(AS3 f4_t*)(L_KVS + (2 * T + q) * 4096 + tid * 16) = o;
    const unsigned za = row == 0 ? L_ZS + w * 256 + h * 64 : L_DUMP + tid * 16;
#pragma unroll
    for (int j = 2 * q; j < 2 * q + 2; ++j) *(AS3 f4_t*)(za + T * 128 + j * 16) = f4_t{kz[4 * j], kz[4 * j + 1], kz[4 * j + 2], kz[4 * j + 3]};
  };
  if (TYPE) {
#pragma unroll
    for (int p = 0; p < UPF; ++p) hpair(p, 4 * I.m);
  } else {
#pragma unroll
    for (int q = 0; q < 4; ++q) { E[4 + q] = pk8(old[2 + (q >> 1)], q & 1); PIN(E[4 + q]); }
    if (UPA) {
#pragma unroll
      for (int p = 0; p < 8; ++p) ppair(p);
      kvmm(0);
#pragma unroll
      for (int p = 8; p < 16; ++p) ppair(p);
      kvst(0, 0); kvst(0, 1);
      kvmm(1);
      kvst(1, 0); kvst(1, 1);
    }
  }
  STAMP(24);
  auto a2 = [&](int ks, int r) {
    return r < 4 ? slo(ufull(ks)) + (ks & 7) * 2048 + (r & 1) * 512 + ((r & 2) ? tA1 : tA0) : slo(ks < 16 ? 17 : 20) + (ks & 15) * 1024 + (r & 1) * 256 + tH;
  };
  bf8_t G[2];
  mv<15, 16>(tA0); mv<15, 16>(tA1);
  PIN(tH);
#pragma unroll
  for (int r = 0; r < (TYPE ? 6 : 4); ++r) W[0][r] = ldstr(a2(0, r));
  unsigned gb = mb + ((((lane >> 4) ^ lane) & 1) ? L_B1 - L_GATE : 0);
  PIN(gb);
  f4_t ga = {};
  if (!TYPE) G[0] = lds128(gb);
#pragma unroll
  for (int t = 0; t < 3; ++t) acc[t] = TYPE ? bias(L_B1 + (4 + t) * 128) : eps;
  const unsigned fv = (lane & 3) * 4, fs = WS_HF + ptile * FST + I.rh * (16 + FRH), fv2 = (lane & 7) * 4, fs2 = WS_HF + ctile * FST + 256;
  unsigned fval = 0;
  u4_t H[36][2];
  auto hload = [&](auto gc) {
    constexpr int gi = gc.value;
    const unsigned so = (unit_tile(gunit(gi), I.r2, I.rh) + (gi < 28 ? gi & 2 : 0)) * 2048;
#pragma unroll
    for (int q = 0; q < 2; ++q) H[gi][q] = __builtin_amdgcn_raw_buffer_load_b128(ws, (gi < 28 ? pv : cv) + (gi & 1) * 2048 + q * 1024, so, gi < 28 ? HLD : OLD);
  };
  SB();
  sfor<96>([&](auto ic) {
    constexpr int ks = ic.value / 3, t = ic.value % 3, cb = ks & 1, nb = cb ^ 1, n = ks + 1;
    {
      if (TYPE || t < 2) { acc[t] = MFMA(cat(W[cb][2 * t], W[cb][2 * t + 1]), xf[ks], acc[t]); PIN(acc[t]); }
      else { ga = __builtin_amdgcn_mfma_f32_16x16x32_bf16(G[cb], xf[ks], ga, 0, 0, 0); PIN(ga); }
      SB();
      if (t == 0 && (TYPE ? (ks & 7) == 7 : (ks == (UPA ? 0 : 7) || ks == 11 || (!OUP && ks == 31)))) { bar(); STAMP(10 + (ks >> 3)); }
      if (!TYPE && ic.value == 0) {
      f4_t s = ldsf4(L_KVS + w * 4096 + lane * 16);
      float z = *(AS3 float*)(L_ZS + w * 64 + (lane & 15) * 4);
#pragma unroll
      for (int q = 1; q < 4; ++q) { s += ldsf4(L_KVS + w * 4096 + q * 1024 + lane * 16); z += *(AS3 float*)(L_ZS + q * 256 + w * 64 + (lane & 15) * 4); }
      __builtin_amdgcn_raw_buffer_store_b64(u2_t{pk(s[0], s[1]), pk(s[2], s[3])}, ws, w * 512 + lane * 8, WS_KVP + I.m * 65536 + I.i * 4096, SC1);
      __builtin_amdgcn_raw_buffer_store_b32(__builtin_bit_cast(unsigned, z), ws, (lane > 15 ? OOB : 2048) + w * 64 + lane * 4, WS_KVP + I.m * 65536 + I.i * 4096, SC1);
      SB();
      }
      if (!TYPE && t == 2 && ks == KFK) { vmcnt<0>(); __builtin_amdgcn_raw_buffer_store_b32(atag, ws, l0, WS_KVF + (I.m * 4 + w) * 128 + I.i * 4, SC1); STAMP(29); }
      if (n < 32) {
        if (t == 0) { mv<ufull(ks), ufull(n)>(tA0); mv<ufull(ks), ufull(n)>(tA1); if (TYPE && n == 16) mv<17, 20>(tH); }
        if (t == 0) { W[nb][0] = ldstr(a2(n, 0)); W[nb][1] = ldstr(a2(n, 1)); W[nb][2] = ldstr(a2(n, 2)); }
        if (t == 1) {
          W[nb][3] = ldstr(a2(n, 3));
          if (TYPE) { W[nb][4] = ldstr(a2(n, 4)); W[nb][5] = ldstr(a2(n, 5)); } else G[nb] = lds128(gb + n * 32);
        }
      }
      if (TYPE) {
        if (ic.value % HSP == 0 && ic.value / HSP < 32 - UPF) hpair((UPF + ic.value / HSP) & 31, 4 * I.m);
        if (t == 2 && ks == EFK) { vmcnt<0>(); __builtin_amdgcn_raw_buffer_store_b32(rtag, ws, l0, WS_HF + ptile * FST + I.m * 4 + I.rh * FRH, SC1); STAMP(29); }
      } else if (!UPA && ks < 11) {
        if (t < 2 && ks < 4) ppair((2 * ks + t) & 15);
        if (t < 2 && ks >= 5 && ks < 9) ppair((2 * ks + t - 2) & 15);
        if (t == 0 && (ks == 4 || ks == 9)) kvmm(ks == 9);
        if (t == 2 && (ks == 4 || ks == 9)) kvst(ks == 9, 0);
        if (t == 2 && ks == 5) kvst(0, 1);
        if (t == 0 && ks == 10) kvst(1, 1);
      }
      if (t == 2 && (TYPE || HB < 0)) {
        if (ks == 27 - PDIST) fval = ld32(ws, fv, fs);
        if (ks == 32 - PDIST) { waitflag(dws, fv, fs, fval, rtag); STAMP(25); }
        if (ks >= 32 - PDIST) hload(std::integral_constant<int, (ks + PDIST) & 31>{});
      }
      SB();
    }
  });
#pragma unroll
  for (int t = 0; t < 3; ++t) old[t] = acc[t];
  auto ocomp = [&]() {
    const u4_t z4 = {};
#pragma unroll
    for (int T = 0; T < 2; ++T) {
      const bf8_t p0 = asbf(E[2 * T]), p1 = asbf(E[2 * T + 1]);
      const u4_t kf = *(AS3 u4_t*)(L_KVF + T * 1024 + lane * 16);
      f16_t num = MFMA(asbf(hi ? z4 : kf), p0, zero);
      const f16_t d0 = MFMA(lds128(L_ZF + T * 64 + h * 32), p0, zero);
      num = MFMA(asbf(hi ? kf : z4), p1, num);
      const f16_t d1 = MFMA(lds128(L_ZF + T * 64 + h * 32 + 16), p1, zero);
      const float r0 = __builtin_amdgcn_rcpf(d0[0] + 1e-6f), r1 = __builtin_amdgcn_rcpf(d1[0] + 1e-6f);
#pragma unroll
      for (int r = 0; r < 16; ++r) num[r] *= r < 8 ? r0 : r1;
      __builtin_amdgcn_raw_buffer_store_b128(pk8(num, 0), ws, pv, (56 + 2 * I.m + T) * 2048, SC1);
      __builtin_amdgcn_raw_buffer_store_b128(pk8(num, 1), ws, pv + 1024, (56 + 2 * I.m + T) * 2048, SC1);
    }
  };
  auto gstore = [&]() {
        const float g = __builtin_amdgcn_rcpf(1.f + __builtin_amdgcn_exp2f((hi ? ga[1] : ga[0]) * -1.44269504088896f));
        __builtin_amdgcn_raw_buffer_store_b32(__builtin_bit_cast(unsigned, g), ws, lane * 4, WS_G + (ptile * 8 + I.m) * 256, SC1);
  };
  if (TYPE && UPL) {
#pragma unroll
    for (int p = 0; p < 24; ++p) hpair(p, 32 + 3 * I.m);
  }
  if (!TYPE && UPQ) {
#pragma unroll
    for (int p = 0; p < 16; ++p) ppair(p);
    if (OUP) {
      gstore();
      if (HB >= 0) { waitflag(dws, fv, fs, rtag + 1, rtag); STAMP(25); }
      bar();
      STAMP(13);
      sfor<PDIST>([&](auto vc) { if (vc.value < HB) hload(vc); });
      SB();
      ocomp();
      SB();
      sfor<PDIST>([&](auto vc) { if (HB >= 0 && vc.value >= HB) hload(vc); });
    }
  }
  STAMP(26);
  unsigned tB0 = shi(22) + (h * 4 + r4) * 128 + 64 * tsw + trc, tB1 = tB0 ^ 64;
  PIN(tB0); PIN(tB1);
  auto a3 = [&](int gi, int r) { return slo(22 + gunit(gi)) + (gi & (gi < 28 ? 3 : 1)) * 4096 + (r >> 2) * 2048 + (r & 1) * 1024 + ((r & 2) ? tB1 : tB0); };
#pragma unroll
  for (int r = 0; r < 8; ++r) W[0][r] = ldstr(a3(0, r));
  const bool keep = (w >> 1) == I.rh;
  const unsigned xs = WS_X + (I.i * 8 + I.c) * 32768 + w * 8192;
  const unsigned xv = lane * 16 + (keep ? OOB : 0);
  acc[0] = bias(L_B2);
  acc[1] = bias(L_B2 + 128);
#pragma unroll
  for (int r = 0; r < 16; ++r) { acc[0][r] = keep ? acc[0][r] : 0.f; acc[1][r] = keep ? acc[1][r] : 0.f; }
  acc[2] = zero;
  acc[3] = zero;
  unsigned gbits = 0;
  SB();
  sfor<144>([&](auto ic) {
    constexpr int gi = ic.value >> 2, t = ic.value & 3, cb = gi & 1, nb = cb ^ 1, n = gi + 1, ab = gi < 28 ? 0 : 2;
    {
      acc[ab + (t & 1)] = MFMA(cat(W[cb][2 * t], W[cb][2 * t + 1]), asbf(H[gi][t >> 1]), acc[ab + (t & 1)]);
      PIN(acc[ab + (t & 1)]);
      SB();
      if (t == 0 && (gi & 3) == 3 && n < 36) { bar(); STAMP(14 + (gi >> 2)); }
      if (n < 36) {
        if (t == 0) { mv<22 + gunit(gi), 22 + gunit(n)>(tB0); mv<22 + gunit(gi), 22 + gunit(n)>(tB1); }
        if (t == 0 && n == 28) { tB0 += (w >> 1) * 8192; tB1 += (w >> 1) * 8192; PIN(tB0); PIN(tB1); }
        if (t == 0) { W[nb][0] = ldstr(a3(n, 0)); W[nb][1] = ldstr(a3(n, 1)); W[nb][2] = ldstr(a3(n, 2)); }
        if (t == 1) { W[nb][3] = ldstr(a3(n, 3)); W[nb][4] = ldstr(a3(n, 4)); W[nb][5] = ldstr(a3(n, 5)); }
        if (t == 2) { W[nb][6] = ldstr(a3(n, 6)); W[nb][7] = ldstr(a3(n, 7)); }
      }
      if (TYPE && !UPL && ic.value < 16) {
        constexpr int p0 = (ic.value & 15) * 3 / 2, p1 = ((ic.value & 15) + 1) * 3 / 2;
        hpair(p0, 32 + 3 * I.m);
        if (p1 - p0 == 2) hpair(p0 + 1, 32 + 3 * I.m);
      }
      if (!TYPE && !UPQ && ic.value < 16) ppair(ic.value & 15);
      if (!TYPE && !OUP && ic.value == 16) {
        gstore();
      }
      if (gi >= 28 && gi < 30) {
        constexpr int T = gi & 1;
        __builtin_amdgcn_raw_buffer_store_b128(u4_t{pk(acc[T][4 * t], acc[T][4 * t + 1]), rtag, pk(acc[T][4 * t + 2], acc[T][4 * t + 3]), rtag}, ws, xv + t * 1024, xs + T * 4096, SC1);
      }
      if (gi == XLG && t == 0) { const unsigned xa_ = xaddr(I, w * 64 + lane); X[0] = __builtin_amdgcn_raw_buffer_load_b128(ws, xa_, 0, SC1); X[1] = __builtin_amdgcn_raw_buffer_load_b128(ws, xa_ + 512, 0, SC1); }
      if (t == 3) {
        if (!TYPE && !OUP && gi == OGI) { ocomp(); SB(); STAMP(22); }
        if (gi == (TYPE ? LFG : OGI + OFD)) { vmcnt<TYPE ? nvm(UPL ? 0 : 3, LFG) : nvm(OGI, OGI + OFD) + (HB >= 0 ? 2 * (PDIST - HB) : 0)>(); __builtin_amdgcn_raw_buffer_store_b32(rtag, ws, l0, WS_HF + ptile * FST + I.m * 4 + (TYPE ? 128 + I.rh * FRH : 256), SC1); }
        if (gi == hissue(16)) waitflag(dws, fv, fs + 128, fval, rtag);
        if (gi == hissue(28)) waitflag(dws, fv2, fs2, fval, rtag);
        if (gi == hissue(16) - 3) fval = ld32(ws, fv, fs + 128);
        if (gi == hissue(28) - 3) fval = ld32(ws, fv2, fs2);
        sfor<36>([&](auto vc) { if (hissue(vc.value) == gi) hload(vc); });
        if (gi == hissue(35)) gbits = ld32(ws, lane * 4, WS_G + (ctile * 8 + I.c) * 256);
      }
      SB();
    }
  });
  STAMP(27);
  if (XLG == 36) { const unsigned xa_ = xaddr(I, w * 64 + lane); X[0] = __builtin_amdgcn_raw_buffer_load_b128(ws, xa_, 0, SC1); X[1] = __builtin_amdgcn_raw_buffer_load_b128(ws, xa_ + 512, 0, SC1); }
  SB();
  const float g = __builtin_bit_cast(float, gbits), g1 = keep ? 1.f - g : 0.f;
  if (!keep) { acc[0] = zero; acc[1] = zero; }
  *(AS3 u2_t*)(L_RED + ((keep ? 0 : 64) + (w & 1) * 32 + row) * 272 + 256) = u2_t{__builtin_bit_cast(unsigned, g1), rtag};
#pragma unroll
  for (int T = 0; T < 2; ++T)
#pragma unroll
    for (int q = 0; q < 4; ++q) {
      f4_t o;
#pragma unroll
      for (int j = 0; j < 4; ++j) { float y = g * acc[2 + T][4 * q + j]; asm("" : "+v"(y)); o[j] = __builtin_fmaf(g1, acc[T][4 * q + j], y); }
      *(AS3 f4_t*)(L_RED + ((w >> 1) * 64 + (w & 1) * 32 + row) * 272 + T * 128 + q * 32 + h * 16) = o;
    }
  SB();
  if (XLG == 37) { const unsigned xa_ = xaddr(I, w * 64 + lane); X[0] = __builtin_amdgcn_raw_buffer_load_b128(ws, xa_, 0, SC1); X[1] = __builtin_amdgcn_raw_buffer_load_b128(ws, xa_ + 512, 0, SC1); }
}

__global__ __launch_bounds__(512, 2) void fused_block(Params P) {
  __shared__ __attribute__((aligned(1024))) char lds[163840];
  asm volatile("" ::"v"((AS3 char*)lds) : "memory");
  const int b = blockIdx.x, xcd = b & 7, s = b >> 3, il = s >> 3, jl = s & 7;
  Ids I;
  I.i = (xcd >> 1) * 4 + il; I.m = (xcd & 1) * 4 + (jl >> 1); I.c = jl; I.rh = xcd & 1; I.rot = ROTX; I.r2 = ROT2;
  const int w = __builtin_amdgcn_readfirstlane(threadIdx.x >> 6), lane = threadIdx.x & 63;
  const rsrc_t ws = __builtin_amdgcn_make_buffer_rsrc(P.ws, 0, WS_END, 0x00020000);
  unsigned prof = 0;
  RSTAMP(30);
  unsigned long long pc;
  asm volatile("s_getpc_b64 %0" : "=s"(pc));
  I.pc = pc & ~63ull; I.s = s;
  u4_t X[2];
  if (w >= 4) {
    if (jl & 1) loader<1>(P, I, w - 4, lane, ws, X, prof); else loader<0>(P, I, w - 4, lane, ws, X, prof);
  } else {
    if (jl & 1) compute<1>(P, I, w, lane, ws, X, prof); else compute<0>(P, I, w, lane, ws, X, prof);
  }
  bar();
  const int t = threadIdx.x >> 3, cq = threadIdx.x & 7;
  const unsigned xa_ = xaddr(I, threadIdx.x);
  const unsigned a = L_RED + t * 272 + cq * 32;
  f4_t v0 = ldsf4(a) + ldsf4(a + 64 * 272), v1 = ldsf4(a + 16) + ldsf4(a + 64 * 272 + 16);
  const u2_t gt = *(AS3 u2_t*)(L_RED + t * 272 + 256);
  for (int n = 0; n < SPIN_MAX; ++n) {
    if (__builtin_amdgcn_ballot_w64(((X[0][1] ^ gt[1]) | (X[0][3] ^ gt[1]) | (X[1][1] ^ gt[1]) | (X[1][3] ^ gt[1])) != 0) == 0) break;
    X[0] = __builtin_amdgcn_raw_buffer_load_b128(ws, xa_, 0, SC1);
    X[1] = __builtin_amdgcn_raw_buffer_load_b128(ws, xa_ + 512, 0, SC1);
    asm volatile("" ::: "memory");
  }
  STAMP(23);
  const float g1 = __builtin_bit_cast(float, gt[0]);
  v0 += g1 * f4_t{bflo(X[0][0]), bfhi(X[0][0]), bflo(X[0][2]), bfhi(X[0][2])};
  v1 += g1 * f4_t{bflo(X[1][0]), bfhi(X[1][0]), bflo(X[1][2]), bfhi(X[1][2])};
  *(u4_t*)(P.out + (I.i * 128 + I.rh * 64 + t) * 1024 + I.c * 128 + cq * 16) = u4_t{pk(v0[0], v0[1]), pk(v0[2], v0[3]), pk(v1[0], v1[1]), pk(v1[2], v1[3])};
  if (threadIdx.x == 0 && I.c == 0 && I.rh == 0) {
    __builtin_amdgcn_raw_buffer_store_b32(gt[1], ws, 0, WS_EP + I.i * 128, SC1);
    if (I.i == 0) __builtin_amdgcn_raw_buffer_store_b32(*(AS3 unsigned*)L_TAG, ws, 0, WS_EP + 2048, SC1);
  }
  STAMP(28);
  RSTAMP(31);
}

extern "C" void kernel_launch(void* const* d_in, const int* in_sizes, int n_in, void* d_out, int out_size, void* d_ws,
                              size_t ws_size, hipStream_t stream) {
  Params P;
  P.x = (const char*)d_in[0]; P.wqkv = (const char*)d_in[1]; P.wo = (const char*)d_in[2]; P.w1 = (const char*)d_in[4];
  P.b1 = (const char*)d_in[5]; P.w2 = (const char*)d_in[6]; P.b2 = (const char*)d_in[7]; P.gw = (const char*)d_in[8];
  P.out = (char*)d_out; P.ws = (char*)d_ws;
  fused_block<<<256, 512, 0, stream>>>(P);
}
```

```cpp
#include <hip/hip_runtime.h>
#include <utility>

typedef __attribute__((ext_vector_type(8))) __bf16 bf8_t;
typedef __attribute__((ext_vector_type(4))) __bf16 bf4_t;
typedef __attribute__((ext_vector_type(2))) __bf16 bf2_t;
typedef __attribute__((ext_vector_type(16))) float f16_t;
typedef __attribute__((ext_vector_type(4))) float f4_t;
typedef __attribute__((ext_vector_type(2))) float f2_t;
typedef __attribute__((ext_vector_type(4))) unsigned u4_t;
typedef __attribute__((ext_vector_type(2))) unsigned u2_t;
typedef __amdgpu_buffer_rsrc_t rsrc_t;
#define AS3 __attribute__((address_space(3)))
#define DI __device__ __forceinline__
#define SB() __builtin_amdgcn_sched_barrier(0)
#define MFMA(a, b, c) __builtin_amdgcn_mfma_f32_32x32x16_bf16((a), (b), (c), 0, 0, 0)
#define SC1 16
#define HST SC1
#define HLD SC1

constexpr unsigned WS_KVP = 0x900000, WS_G = 0x980000, WS_HF = 0x9C0000, WS_KVF = 0x9A8000, WS_EP = 0x9B0000,
                   WS_PROF = 0xA00000, WS_X = 0xB00000, WS_END = 0xF00000;
constexpr unsigned SLOT = 16384, MISC = 9 * SLOT, L_GATE = MISC, L_B1 = MISC + 1024, L_B2 = MISC + 2048,
                   L_ZS = MISC + 2560, L_KVF = MISC + 4096, L_ZF = MISC + 6144, L_TAG = MISC + 6400, L_DUMP = MISC + 8192, L_KVS = 8 * SLOT, L_RED = 6 * SLOT;
#define CB 45440
constexpr unsigned CODE_BYTES = CB;
constexpr int SPIN_MAX = 1 << 17;
#define PDIST 8
#define ROT2 (il + jl)
#define ROTX ((il + (jl >> 1)) & 1)
constexpr int NSTEP = 21;
#define UT1 {5, 7, 9, 11, 13, 15, 17, 18, 20, 22, 24, 25, 26, 27, 28, 29, 30, 31, 32, 32, 32}
#define UT0 {5, 7, 9, 11, 13, 15, 18, 21, 23, 23, 24, 24, 25, 26, 28, 29, 30, 31, 32, 32, 32}
#define SLP 10
#define PRIO 2
#define LGI 9
#define OFG 23
#define XLG 35
#define EFK 14
#define UPF 16
#define UPL 0
#define UPA 1
#define UPQ 1
#define KP (UPA ? 9 : 10)
#define HSP 1
#define FRH 384
#define FST 768
#define PSL "4"
#define SL0 0
#define XSL 8
#define HB -1
#define OLD SC1
#define KFK 4
#define OAUX SC1
#define XE 0
#define PFA ((blockIdx.x & 8) * 2048u + threadIdx.x * 64u)
#define KT 10
#define KS 10
#define LFG 5
#define OFD 3
#define LPRIO 3
constexpr bool OUP = KS == 10;
constexpr int OGI = OUP ? 0 : 4 * (KS - 11);
constexpr int UPRO = 3;
constexpr int uafter(int type, int t) {
  constexpr int U1[NSTEP] = UT1;
  constexpr int U0[NSTEP] = UT0;
  return t < 0 ? UPRO : type ? U1[t] : U0[t];
}
constexpr int uneed(int type, int t) {
  return t < 8 ? 2 * t + 1 : t == 8 ? 17 : t == 9 ? 18 : t == 10 ? (type ? 20 : 21) : t == 11 ? 21 : t < 19 ? 10 + t : 30 + 2 * (t - 19);
}
constexpr bool uskip(int type, int u) { return type == 0 && (u == 17 || u == 20); }
constexpr int vmwait(int type, int t) {
  int n = 0;
  for (int u = uneed(type, t) + 1; u <= uafter(type, t - 1); ++u) n += uskip(type, u) ? 0 : 4;
  if (type == 0 && KS > KT && t == KS) n += 12;
  return n;
}
constexpr int imax(int a, int b) { return a > b ? a : b; }
constexpr int hissue(int v) { return v < 16 ? v - PDIST : v < 28 ? imax(v - PDIST, LGI + (v - 16) / 2) : imax(v - PDIST, OFG + (v - 28) / 2); }
constexpr int nvm(int g0, int g1) { int n = 0; for (int v = 0; v < 36; ++v) n += hissue(v) >= g0 && hissue(v) < g1 ? 2 : 0; return n; }
constexpr unsigned slot(int u) { return (unsigned)(u % 9) * SLOT; }
constexpr unsigned slo(int u) { return slot(u) & 0xffffu; }
constexpr unsigned shi(int u) { return slot(u) & ~0xffffu; }
constexpr int ufull(int ks) { return ks < 8 ? 16 : ks < 16 ? 18 : ks < 24 ? 19 : 21; }

#define RSTAMP(id)
#define STAMP(id)

struct Params {
  const char *x, *wqkv, *wo, *w1, *b1, *w2, *b2, *gw;
  char *out, *ws;
};
struct Ids { int i, m, c, rh, rot, r2; };
DI unsigned xaddr(const Ids& I, unsigned v) { asm volatile("" : "+v"(v)); return WS_X + (I.i * 8 + I.c) * 32768 + (2 * I.rh + (v >> 8)) * 8192 + (v & 7) * 1024 + ((v >> 3) & 31) * 16; }

template <int N> DI void vmcnt() { asm volatile("s_waitcnt vmcnt(%0)" ::"n"(N) : "memory"); }
DI void bar() { asm volatile("s_waitcnt lgkmcnt(0)\n\ts_barrier" ::: "memory"); SB(); }
DI bf8_t lds128(unsigned a) { return *(AS3 bf8_t*)a; }
DI f4_t ldsf4(unsigned a) { return *(AS3 f4_t*)a; }
DI bf4_t ldstr(unsigned a) { return __builtin_amdgcn_ds_read_tr16_b64_v4bf16((AS3 bf4_t*)a); }
DI bf8_t cat(bf4_t a, bf4_t b) { return __builtin_shufflevector(a, b, 0, 1, 2, 3, 4, 5, 6, 7); }
DI unsigned pk(float a, float b) { f2_t f = {a, b}; return __builtin_bit_cast(unsigned, __builtin_convertvector(f, bf2_t)); }
DI float bflo(unsigned v) { return __builtin_bit_cast(float, v << 16); }
DI float bfhi(unsigned v) { return __builtin_bit_cast(float, v & 0xffff0000u); }
DI u4_t pk8(const f16_t& a, int s) { return u4_t{pk(a[8 * s], a[8 * s + 1]), pk(a[8 * s + 2], a[8 * s + 3]), pk(a[8 * s + 4], a[8 * s + 5]), pk(a[8 * s + 6], a[8 * s + 7])}; }
DI bf8_t asbf(u4_t v) { return __builtin_bit_cast(bf8_t, v); }
DI float relu(float t) { return __builtin_bit_cast(float, max(__builtin_bit_cast(int, t), 0)); }
DI float phi(float t) {
  float e = __builtin_amdgcn_fmed3f(__builtin_amdgcn_exp2f(t * 1.44269504088896f), 0.f, 1.f);
  return (relu(t) + e) + 1e-6f;
}
DI float phi1(float t1) {
  float e = __builtin_amdgcn_fmed3f(__builtin_amdgcn_exp2f(__builtin_fmaf(t1, 1.44269504088896f, -1.44269504088896e-6f)), 0.f, 1.f);
  return __builtin_bit_cast(float, max(__builtin_bit_cast(int, t1), __builtin_bit_cast(int, 1e-6f))) + e;
}
typedef __attribute__((ext_vector_type(2))) short s2_t;
DI unsigned relu2pk(float a, float b) {
  float x = a * __builtin_fabsf(a);
  asm("" : "+v"(x));
  const s2_t p = __builtin_bit_cast(s2_t, pk(x, b * __builtin_fabsf(b)));
  return __builtin_bit_cast(unsigned, __builtin_elementwise_max(p, s2_t{0, 0}));
}
DI u4_t mkdesc(const void* p, unsigned bytes) {
  unsigned long long a = (unsigned long long)p;
  return u4_t{(unsigned)a, (unsigned)(a >> 32) & 0xffffu, bytes, 0x00020000u};
}
DI void dma(unsigned ldsa, unsigned voff, u4_t d, unsigned soff) {
  asm volatile("s_mov_b32 m0, %0\n\ts_nop 0\n\tbuffer_load_dwordx4 %1, %2, %3 offen lds" ::"s"(ldsa), "v"(voff), "s"(d), "s"(soff) : "memory");
}
DI unsigned ld32(rsrc_t r, unsigned v, unsigned s) { return __builtin_amdgcn_raw_buffer_load_b32(r, v, s, SC1); }
DI void waitflag(u4_t d, unsigned v, unsigned s, unsigned val, unsigned tag) {
  unsigned n = SPIN_MAX;
  asm volatile("s_nop 4\n1%=:\n\tv_cmp_ne_u32 vcc, %4, %0\n\ts_cbranch_vccz 2%=f\n\ts_sleep " PSL "\n\tbuffer_load_dword %0, %2, %3, %5 offen sc1\n\t"
               "s_sub_u32 %1, %1, 1\n\ts_waitcnt vmcnt(0)\n\ts_cbranch_scc0 1%=b\n2%=:"
               : "+v"(val), "+s"(n) : "v"(v), "s"(d), "s"(tag), "s"(s) : "memory", "vcc", "scc");
}
template <class F, int... T> DI void sfor_(F&& f, std::integer_sequence<int, T...>) { (f(std::integral_constant<int, T>{}), ...); }
template <int N, class F> DI void sfor(F&& f) { sfor_(f, std::make_integer_sequence<int, N>{}); }
#define PIN(x) asm volatile("" : "+v"(x))
template <int FROM, int TO> DI void mv(unsigned& a) { if (shi(FROM) != shi(TO)) { a += shi(TO) - shi(FROM); PIN(a); } }
constexpr unsigned OOB = 0x80000000u;
DI int unit_tile(int u, int c, int rh) { return u < 4 ? 16 * rh + 4 * ((u + c) & 3) : u < 7 ? 32 + 12 * rh + 4 * ((u - 4 + c) % 3) : 56 + 4 * ((u - 7 + c) & 3); }
constexpr int gunit(int gi) { return gi < 28 ? gi >> 2 : 7 + ((gi - 28) >> 1); }

template <int TYPE> DI void loader(const Params& P, const Ids& I, int lw, int lane, unsigned pf, rsrc_t ws, u4_t* X, unsigned& prof) {
  __builtin_amdgcn_s_setprio(LPRIO);
  constexpr unsigned WSTR = TYPE ? 3584 : 3072;
  const u4_t dx = mkdesc(P.x, 2048 * 1024), dw = TYPE ? mkdesc(P.w1, 512 * 3584) : mkdesc(P.wqkv, 512 * 3072);
  const u4_t d2 = mkdesc(P.w2, 1792 * 1024), dwo = mkdesc(P.wo, 512 * 1024);
  const unsigned r8 = lane >> 3, ch = lane & 7, sw = (ch ^ (((lane >> 4) & 1) << 2)) * 16;
  const unsigned vx0 = r8 * 1024 + (ch ^ (lane >> 4)) * 16, vx1 = r8 * 1024 + (ch ^ (4 + (lane >> 4))) * 16;
  const unsigned vw = r8 * WSTR + sw, vh = (lane >> 2) * WSTR + (lane & 3) * 16, v2 = r8 * 1024 + sw;
  const unsigned mo = I.m & 1;
  const unsigned xb = (I.i * 128 + lw * 32) * 1024;
  const unsigned c1 = TYPE ? 448 * I.m + 64 * mo + 128 * (lw >> 1) : 1024 * (1 + (lw >> 1)) + 128 * I.m;
  const unsigned cF = TYPE ? 448 * I.m + 64 * mo + 256 : 128 * I.m, cH = 448 * I.m + (mo ? 0 : 384);
  const unsigned lb = lw * 4096;
  const u4_t dws = mkdesc(P.ws, WS_END);
  const unsigned kfv = (lane & 15) * 4, kfo = WS_KVF + (I.m * 4 + lw) * 128;
  unsigned atag;
  u4_t gk[8];
  float gz[4];
  auto issue = [&](int u) {
    if (uskip(TYPE, u)) return;
    const unsigned l0 = slot(u) + lb;
    if (u < 16) {
      const unsigned kc = ((u >> 1) + I.rot) & 7;
      if ((u & 1) == 0) {
#pragma unroll
        for (int p = 0; p < 4; ++p) dma(l0 + p * 1024, (p & 1) ? vx1 : vx0, dx, xb + kc * 128 + p * 8192);
      } else {
#pragma unroll
        for (int p = 0; p < 4; ++p) dma(l0 + p * 1024, vw, dw, (kc * 64 + (lw & 1) * 32 + p * 8) * WSTR + c1);
      }
    } else if (u < 22) {
      if (u == 17 || u == 20) {
        const unsigned kc = ((u == 17 ? 0 : 4) + lw + I.rot) & 7;
#pragma unroll
        for (int p = 0; p < 4; ++p) dma(l0 + p * 1024, vh, dw, (kc * 64 + p * 16) * WSTR + cH);
      } else {
        const int n = u == 16 ? 0 : u == 18 ? 1 : u == 19 ? 2 : 3;
        const unsigned kc = (2 * n + (lw >> 1) + I.rot) & 7;
#pragma unroll
        for (int p = 0; p < 4; ++p) dma(l0 + p * 1024, vw, dw, (kc * 64 + (lw & 1) * 32 + p * 8) * WSTR + cF);
      }
    } else {
      const int q = u - 22;
      const unsigned ts = unit_tile(q, I.r2, I.rh) + lw;
      unsigned row;
      if (q < 4) { const unsigned mp = ts >> 2; row = 224 * mp + 32 * (mp & 1) + 32 * lw; }
      else if (q < 7) {
        const unsigned tt = ts - 32, mp = (tt * 43) >> 7, t3 = tt - 3 * mp;
        row = 224 * mp + ((mp & 1) ? (t3 < 2 ? 160 + 32 * t3 : 0) : 128 + 32 * t3);
      } else row = 32 * (ts - 56);
#pragma unroll
      for (int p = 0; p < 4; ++p) dma(l0 + p * 1024, v2, q < 7 ? d2 : dwo, (row + p * 8) * 1024 + I.c * 128);
    }
  };
  STAMP(0);
#pragma unroll
  for (int u = 0; u <= UPRO; ++u) { if (u == 2 && SL0) __builtin_amdgcn_s_sleep(SL0); issue(u); }
  auto step = [&](auto tc) {
    constexpr int t = decltype(tc)::value;
    if (TYPE == 0 && t == 11) return;
    vmcnt<vmwait(TYPE, t)>();
    if (t == 0) asm volatile("" ::"v"(pf));
    STAMP(33 + t);
    bar();
    STAMP(1 + t);
    const unsigned pb = WS_KVP + I.m * 65536;
    if (TYPE == 0 && t == 0) atag = __builtin_amdgcn_readfirstlane(*(AS3 unsigned*)L_TAG);
    if (TYPE == 0 && t == KT) {
      waitflag(dws, kfv, kfo, atag + 1, atag);
      STAMP(24);
      const unsigned gv = lw * 512 + (lane & 31) * 16 + (lane >> 5) * 32768, zv = 2048 + lw * 64 + (lane >> 4) * 4096 + (lane & 15) * 4;
#pragma unroll
      for (int r = 0; r < 8; ++r) asm volatile("buffer_load_dwordx4 %0, %1, %2, %3 offen sc1" : "=v"(gk[r]) : "v"(gv), "s"(dws), "s"(pb + r * 4096) : "memory");
#pragma unroll
      for (int r = 0; r < 4; ++r) asm volatile("buffer_load_dword %0, %1, %2, %3 offen sc1" : "=v"(gz[r]) : "v"(zv), "s"(dws), "s"(pb + r * 16384) : "memory");
    }
    if (TYPE && (t == 8 || t == 12)) __builtin_amdgcn_s_sleep(SLP);
#pragma unroll
    for (int u = uafter(TYPE, t - 1) + 1; u <= uafter(TYPE, t); ++u) issue(u);
    if (TYPE == 0 && t == KS) {
      vmcnt<4 * (uafter(0, KS) - uafter(0, KT - 1))>();
      STAMP(26);
      float sm[8] = {};
#pragma unroll
      for (int r = 0; r < 8; ++r) {
        PIN(gk[r]);
#pragma unroll
        for (int k = 0; k < 8; ++k) sm[k] += k & 1 ? bfhi(gk[r][k >> 1]) : bflo(gk[r][k >> 1]);
      }
#pragma unroll
      for (int k = 0; k < 4; ++k) {
        asm volatile("s_nop 4\n\tv_permlane32_swap_b32 %0, %1\n\ts_nop 1" : "+v"(sm[k]), "+v"(sm[4 + k]));
        sm[k] += sm[4 + k];
      }
      *(AS3 u2_t*)(L_KVF + (lw >> 1) * 1024 + (2 * (lane & 31) + (lane >> 5)) * 16 + (lw & 1) * 8) = u2_t{pk(sm[0], sm[1]), pk(sm[2], sm[3])};
#pragma unroll
      for (int r = 0; r < 4; ++r) PIN(gz[r]);
      float z = (gz[0] + gz[1]) + (gz[2] + gz[3]);
      z += __shfl_xor(z, 16);
      z += __shfl_xor(z, 32);
      *(AS3 unsigned short*)(L_ZF + lw * 32 + (lane & 15) * 2) = (unsigned short)pk(z, z);
      STAMP(25);
    }
  };
  sfor<NSTEP>(step);
  __builtin_amdgcn_s_sleep(XSL);
  const unsigned xa_ = xaddr(I, lw * 64 + 256 + lane);
  X[0] = __builtin_amdgcn_raw_buffer_load_b128(ws, xa_, 0, SC1);
  X[1] = __builtin_amdgcn_raw_buffer_load_b128(ws, xa_ + 512, 0, SC1);
}

template <int TYPE> DI void compute(const Params& P, const Ids& I, int w, int lane, unsigned pf, rsrc_t ws, u4_t* X, unsigned& prof) {
  __builtin_amdgcn_s_setprio(PRIO);
  const int tid = w * 64 + lane, h = lane >> 5, mo = I.m & 1;
  STAMP(0);
  const unsigned e0 = ld32(ws, 0, WS_EP + I.i * 128), e1 = ld32(ws, 0, WS_EP + 2048);
  const unsigned m0 = TYPE ? *(const unsigned short*)(P.b1 + (224 * I.m + (mo ? (tid < 192 ? 32 + tid : tid - 192) : min(tid, 223))) * 2)
                           : *(const unsigned*)(P.gw + ((tid * 4 + I.rot * 128) & 1023));
  const unsigned m1 = *(const unsigned short*)(P.b2 + (64 * I.c + lane) * 2);
  SB();
  const unsigned r4 = (lane >> 2) & 3, trc = ((lane >> 4) & 1) * 32 + (lane & 3) * 8, tsw = (r4 >> 1) & 1;
  unsigned tA0 = h * 1024 + r4 * 128 + 64 * tsw + trc, tA1 = h * 1024 + r4 * 128 + 64 * (tsw ^ 1) + trc;
  unsigned tH = h * 512 + r4 * 64 + trc + shi(17), mb = MISC + h * 16;
  PIN(mb);
  const unsigned row = lane & 31;
  unsigned xa[4];
#pragma unroll
  for (int s = 0; s < 4; ++s) xa[s] = w * 4096 + row * 128 + ((2 * s + h) ^ ((row >> 1) & 7)) * 16;
  const unsigned ptile = I.i * 4 + w;
  const unsigned pv = ptile * (72 * 2048) + lane * 16;
  const unsigned ctile = I.i * 4 + I.rh * 2 + (w & 1);
  const unsigned cv = ctile * (72 * 2048) + (w >> 1) * 4096 + lane * 16;
  auto bias = [&](unsigned a) {
    f16_t r;
#pragma unroll
    for (int q = 0; q < 4; ++q) { f4_t b = ldsf4(mb + (a - MISC) + q * 32); r[4 * q] = b[0]; r[4 * q + 1] = b[1]; r[4 * q + 2] = b[2]; r[4 * q + 3] = b[3]; }
    return r;
  };
  const f16_t zero = {};
  const f16_t eps = zero + 1e-6f;
  const u4_t dws = mkdesc(P.ws, WS_END);
  const unsigned l0 = lane ? OOB : 0;
  SB();
  *(AS3 unsigned*)((TYPE ? L_B1 : L_GATE) + tid * 4) = TYPE ? m0 << 16 : m0;
  *(AS3 unsigned*)(L_B2 + lane * 4) = m1 << 16;
  const unsigned rtag = __builtin_amdgcn_readfirstlane(e0) + 1, atag = __builtin_amdgcn_readfirstlane(e1) + 1;
  if (!TYPE) { *(AS3 unsigned*)L_TAG = atag; *(AS3 unsigned*)(L_B1 + tid * 4) = 0; }
  asm volatile("" ::"s"(rtag), "s"(atag), "v"(pf));
  bar();
  STAMP(1);
  bf8_t xf[32];
  bf4_t W[2][8];
  f16_t acc[4];
  auto a1 = [&](int ks, int r) { return slo(2 * (ks >> 2) + 1) + (r >> 2) * 8192 + (ks & 3) * 2048 + (r & 1) * 512 + ((r & 2) ? tA1 : tA0); };
  xf[0] = lds128(slot(0) + xa[0]);
#pragma unroll
  for (int r = 0; r < 8; ++r) W[0][r] = ldstr(a1(0, r));
#pragma unroll
  for (int t = 0; t < 4; ++t) acc[t] = TYPE ? bias(L_B1 + t * 128) : t < 2 ? eps : zero;
  SB();
  sfor<128>([&](auto ic) {
    constexpr int ks = ic.value >> 2, t = ic.value & 3, cb = ks & 1, nb = cb ^ 1, n = ks + 1;
    {
      const bf8_t wf = cat(W[cb][2 * t], W[cb][2 * t + 1]);
      acc[t] = TYPE ? MFMA(wf, xf[ks], acc[t]) : MFMA(xf[ks], wf, acc[t]);
      PIN(acc[t]);
      SB();
      if (t == 0 && (ks & 3) == 3) { bar(); STAMP(2 + (ks >> 2)); }
      if (n < 32) {
        if (t == 0) {
          if (n >= 4) mv<2 * (n >> 2) - 2, 2 * (n >> 2)>(xa[n & 3]);
          if ((n & 3) == 0) { mv<2 * (n >> 2) - 1, 2 * (n >> 2) + 1>(tA0); mv<2 * (n >> 2) - 1, 2 * (n >> 2) + 1>(tA1); }
        }
        if (t == 0) { xf[n & 31] = lds128(slo(2 * (n >> 2)) + xa[n & 3]); W[nb][0] = ldstr(a1(n, 0)); W[nb][1] = ldstr(a1(n, 1)); }
        if (t == 1) { W[nb][2] = ldstr(a1(n, 2)); W[nb][3] = ldstr(a1(n, 3)); W[nb][4] = ldstr(a1(n, 4)); }
        if (t == 2) { W[nb][5] = ldstr(a1(n, 5)); W[nb][6] = ldstr(a1(n, 6)); W[nb][7] = ldstr(a1(n, 7)); }
        SB();
      }
    }
  });
  f16_t old[4];
  u4_t E[8];
#pragma unroll
  for (int t = 0; t < 4; ++t) old[t] = acc[t];
  auto hpair = [&](int p, unsigned ts0) {
    unsigned v = relu2pk(old[p >> 3][2 * (p & 7)], old[p >> 3][2 * (p & 7) + 1]);
    PIN(v);
    E[p >> 2][p & 3] = v;
    if ((p & 3) == 3) __builtin_amdgcn_raw_buffer_store_b128(E[p >> 2], ws, pv + ((p >> 2) & 1) * 1024, (ts0 + (p >> 3)) * 2048, HST);
  };
  auto ppair = [&](int p) {
    float x = phi1(old[p >> 3][2 * (p & 7)]);
    asm("" : "+v"(x));
    unsigned v = pk(x, phi1(old[p >> 3][2 * (p & 7) + 1]));
    PIN(v);
    E[p >> 2][p & 3] = v;
  };
  const bool hi = lane & 16;
  f16_t kv, kz;
  auto kvmm = [&](int T) {
    const bf8_t ones = asbf(u4_t{0x3f803f80u, 0x3f803f80u, 0x3f803f80u, 0x3f803f80u});
    kv = MFMA(asbf(E[2 * T]), asbf(E[4 + 2 * T]), zero);
    kz = MFMA(asbf(E[2 * T]), ones, zero);
    kv = MFMA(asbf(E[2 * T + 1]), asbf(E[5 + 2 * T]), kv);
    kz = MFMA(asbf(E[2 * T + 1]), ones, kz);
    PIN(kv); PIN(kz);
  };
  auto kvst = [&](int T, int q) {
    f4_t o;
#pragma unroll
    for (int j = 0; j < 4; ++j) { float lo_ = kv[4 * q + j]; PIN(lo_); o[j] = hi ? kv[8 + 4 * q + j] : lo_; }
    *(AS3 f4_t*)(L_KVS + (2 * T + q) * 4096 + tid * 16) = o;
    const unsigned za = row == 0 ? L_ZS + w * 256 + h * 64 : L_DUMP + tid * 16;
#pragma unroll
    for (int j = 2 * q; j < 2 * q + 2; ++j) *(AS3 f4_t*)(za + T * 128 + j * 16) = f4_t{kz[4 * j], kz[4 * j + 1], kz[4 * j + 2], kz[4 * j + 3]};
  };
  if (TYPE) {
#pragma unroll
    for (int p = 0; p < UPF; ++p) hpair(p, 4 * I.m);
  } else {
#pragma unroll
    for (int q = 0; q < 4; ++q) { E[4 + q] = pk8(old[2 + (q >> 1)], q & 1); PIN(E[4 + q]); }
    if (UPA) {
#pragma unroll
      for (int p = 0; p < 8; ++p) ppair(p);
      kvmm(0);
#pragma unroll
      for (int p = 8; p < 16; ++p) ppair(p);
      kvst(0, 0); kvst(0, 1);
      kvmm(1);
      kvst(1, 0); kvst(1, 1);
    }
  }
  STAMP(24);
  auto a2 = [&](int ks, int r) {
    return r < 4 ? slo(ufull(ks)) + (ks & 7) * 2048 + (r & 1) * 512 + ((r & 2) ? tA1 : tA0) : slo(ks < 16 ? 17 : 20) + (ks & 15) * 1024 + (r & 1) * 256 + tH;
  };
  bf8_t G[2];
  mv<15, 16>(tA0); mv<15, 16>(tA1);
  PIN(tH);
#pragma unroll
  for (int r = 0; r < (TYPE ? 6 : 4); ++r) W[0][r] = ldstr(a2(0, r));
  unsigned gb = mb + ((((lane >> 4) ^ lane) & 1) ? L_B1 - L_GATE : 0);
  PIN(gb);
  f4_t ga = {};
  if (!TYPE) G[0] = lds128(gb);
#pragma unroll
  for (int t = 0; t < 3; ++t) acc[t] = TYPE ? bias(L_B1 + (4 + t) * 128) : eps;
  const unsigned fv = (lane & 3) * 4, fs = WS_HF + ptile * FST + I.rh * (16 + FRH), fv2 = (lane & 7) * 4, fs2 = WS_HF + ctile * FST + 256;
  unsigned fval = 0;
  u4_t H[36][2];
  auto hload = [&](auto gc) {
    constexpr int gi = gc.value;
    const unsigned so = (unit_tile(gunit(gi), I.r2, I.rh) + (gi < 28 ? gi & 2 : 0)) * 2048;
#pragma unroll
    for (int q = 0; q < 2; ++q) H[gi][q] = __builtin_amdgcn_raw_buffer_load_b128(ws, (gi < 28 ? pv : cv) + (gi & 1) * 2048 + q * 1024, so, gi < 28 ? HLD : OLD);
  };
  SB();
  sfor<96>([&](auto ic) {
    constexpr int ks = ic.value / 3, t = ic.value % 3, cb = ks & 1, nb = cb ^ 1, n = ks + 1;
    {
      if (TYPE || t < 2) { acc[t] = MFMA(cat(W[cb][2 * t], W[cb][2 * t + 1]), xf[ks], acc[t]); PIN(acc[t]); }
      else { ga = __builtin_amdgcn_mfma_f32_16x16x32_bf16(G[cb], xf[ks], ga, 0, 0, 0); PIN(ga); }
      SB();
      if (t == 0 && (TYPE ? (ks & 7) == 7 : (ks == (UPA ? 0 : 7) || ks == 11 || (!OUP && ks == 31)))) { bar(); STAMP(10 + (ks >> 3)); }
      if (!TYPE && ic.value == 0) {
      f4_t s = ldsf4(L_KVS + w * 4096 + lane * 16);
      float z = *(AS3 float*)(L_ZS + w * 64 + (lane & 15) * 4);
#pragma unroll
      for (int q = 1; q < 4; ++q) { s += ldsf4(L_KVS + w * 4096 + q * 1024 + lane * 16); z += *(AS3 float*)(L_ZS + q * 256 + w * 64 + (lane & 15) * 4); }
      __builtin_amdgcn_raw_buffer_store_b64(u2_t{pk(s[0], s[1]), pk(s[2], s[3])}, ws, w * 512 + lane * 8, WS_KVP + I.m * 65536 + I.i * 4096, SC1);
      __builtin_amdgcn_raw_buffer_store_b32(__builtin_bit_cast(unsigned, z), ws, (lane > 15 ? OOB : 2048) + w * 64 + lane * 4, WS_KVP + I.m * 65536 + I.i * 4096, SC1);
      SB();
      }
      if (!TYPE && t == 2 && ks == KFK) { vmcnt<0>(); __builtin_amdgcn_raw_buffer_store_b32(atag, ws, l0, WS_KVF + (I.m * 4 + w) * 128 + I.i * 4, SC1); STAMP(29); }
      if (n < 32) {
        if (t == 0) { mv<ufull(ks), ufull(n)>(tA0); mv<ufull(ks), ufull(n)>(tA1); if (TYPE && n == 16) mv<17, 20>(tH); }
        if (t == 0) { W[nb][0] = ldstr(a2(n, 0)); W[nb][1] = ldstr(a2(n, 1)); W[nb][2] = ldstr(a2(n, 2)); }
        if (t == 1) {
          W[nb][3] = ldstr(a2(n, 3));
          if (TYPE) { W[nb][4] = ldstr(a2(n, 4)); W[nb][5] = ldstr(a2(n, 5)); } else G[nb] = lds128(gb + n * 32);
        }
      }
      if (TYPE) {
        if (ic.value % HSP == 0 && ic.value / HSP < 32 - UPF) hpair((UPF + ic.value / HSP) & 31, 4 * I.m);
        if (t == 2 && ks == EFK) { vmcnt<0>(); __builtin_amdgcn_raw_buffer_store_b32(rtag, ws, l0, WS_HF + ptile * FST + I.m * 4 + I.rh * FRH, SC1); STAMP(29); }
      } else if (!UPA && ks < 11) {
        if (t < 2 && ks < 4) ppair((2 * ks + t) & 15);
        if (t < 2 && ks >= 5 && ks < 9) ppair((2 * ks + t - 2) & 15);
        if (t == 0 && (ks == 4 || ks == 9)) kvmm(ks == 9);
        if (t == 2 && (ks == 4 || ks == 9)) kvst(ks == 9, 0);
        if (t == 2 && ks == 5) kvst(0, 1);
        if (t == 0 && ks == 10) kvst(1, 1);
      }
      if (t == 2 && (TYPE || HB < 0)) {
        if (ks == 27 - PDIST) fval = ld32(ws, fv, fs);
        if (ks == 32 - PDIST) { waitflag(dws, fv, fs, fval, rtag); STAMP(25); }
        if (ks >= 32 - PDIST) hload(std::integral_constant<int, (ks + PDIST) & 31>{});
      }
      SB();
    }
  });
#pragma unroll
  for (int t = 0; t < 3; ++t) old[t] = acc[t];
  auto ocomp = [&]() {
    const u4_t z4 = {};
#pragma unroll
    for (int T = 0; T < 2; ++T) {
      const bf8_t p0 = asbf(E[2 * T]), p1 = asbf(E[2 * T + 1]);
      const u4_t kf = *(AS3 u4_t*)(L_KVF + T * 1024 + lane * 16);
      f16_t num = MFMA(asbf(hi ? z4 : kf), p0, zero);
      const f16_t d0 = MFMA(lds128(L_ZF + T * 64 + h * 32), p0, zero);
      num = MFMA(asbf(hi ? kf : z4), p1, num);
      const f16_t d1 = MFMA(lds128(L_ZF + T * 64 + h * 32 + 16), p1, zero);
      const float r0 = __builtin_amdgcn_rcpf(d0[0] + 1e-6f), r1 = __builtin_amdgcn_rcpf(d1[0] + 1e-6f);
#pragma unroll
      for (int r = 0; r < 16; ++r) num[r] *= r < 8 ? r0 : r1;
      __builtin_amdgcn_raw_buffer_store_b128(pk8(num, 0), ws, pv, (56 + 2 * I.m + T) * 2048, SC1);
      __builtin_amdgcn_raw_buffer_store_b128(pk8(num, 1), ws, pv + 1024, (56 + 2 * I.m + T) * 2048, SC1);
    }
  };
  auto gstore = [&]() {
        const float g = __builtin_amdgcn_rcpf(1.f + __builtin_amdgcn_exp2f((hi ? ga[1] : ga[0]) * -1.44269504088896f));
        __builtin_amdgcn_raw_buffer_store_b32(__builtin_bit_cast(unsigned, g), ws, lane * 4, WS_G + (ptile * 8 + I.m) * 256, SC1);
  };
  if (TYPE && UPL) {
#pragma unroll
    for (int p = 0; p < 24; ++p) hpair(p, 32 + 3 * I.m);
  }
  if (!TYPE && UPQ) {
#pragma unroll
    for (int p = 0; p < 16; ++p) ppair(p);
    if (OUP) {
      gstore();
      if (HB >= 0) { waitflag(dws, fv, fs, rtag + 1, rtag); STAMP(25); }
      bar();
      STAMP(13);
      sfor<PDIST>([&](auto vc) { if (vc.value < HB) hload(vc); });
      SB();
      ocomp();
      SB();
      sfor<PDIST>([&](auto vc) { if (HB >= 0 && vc.value >= HB) hload(vc); });
    }
  }
  STAMP(26);
  unsigned tB0 = shi(22) + (h * 4 + r4) * 128 + 64 * tsw + trc, tB1 = tB0 ^ 64;
  PIN(tB0); PIN(tB1);
  auto a3 = [&](int gi, int r) { return slo(22 + gunit(gi)) + (gi & (gi < 28 ? 3 : 1)) * 4096 + (r >> 2) * 2048 + (r & 1) * 1024 + ((r & 2) ? tB1 : tB0); };
#pragma unroll
  for (int r = 0; r < 8; ++r) W[0][r] = ldstr(a3(0, r));
  const bool keep = (w >> 1) == I.rh;
  const unsigned xs = WS_X + (I.i * 8 + I.c) * 32768 + w * 8192;
  const unsigned xv = lane * 16 + (keep ? OOB : 0);
  acc[0] = bias(L_B2);
  acc[1] = bias(L_B2 + 128);
#pragma unroll
  for (int r = 0; r < 16; ++r) { acc[0][r] = keep ? acc[0][r] : 0.f; acc[1][r] = keep ? acc[1][r] : 0.f; }
  acc[2] = zero;
  acc[3] = zero;
  unsigned gbits = 0;
  SB();
  sfor<144>([&](auto ic) {
    constexpr int gi = ic.value >> 2, t = ic.value & 3, cb = gi & 1, nb = cb ^ 1, n = gi + 1, ab = gi < 28 ? 0 : 2;
    {
      acc[ab + (t & 1)] = MFMA(cat(W[cb][2 * t], W[cb][2 * t + 1]), asbf(H[gi][t >> 1]), acc[ab + (t & 1)]);
      PIN(acc[ab + (t & 1)]);
      SB();
      if (t == 0 && (gi & 3) == 3 && n < 36) { bar(); STAMP(14 + (gi >> 2)); }
      if (n < 36) {
        if (t == 0) { mv<22 + gunit(gi), 22 + gunit(n)>(tB0); mv<22 + gunit(gi), 22 + gunit(n)>(tB1); }
        if (t == 0 && n == 28) { tB0 += (w >> 1) * 8192; tB1 += (w >> 1) * 8192; PIN(tB0); PIN(tB1); }
        if (t == 0) { W[nb][0] = ldstr(a3(n, 0)); W[nb][1] = ldstr(a3(n, 1)); W[nb][2] = ldstr(a3(n, 2)); }
        if (t == 1) { W[nb][3] = ldstr(a3(n, 3)); W[nb][4] = ldstr(a3(n, 4)); W[nb][5] = ldstr(a3(n, 5)); }
        if (t == 2) { W[nb][6] = ldstr(a3(n, 6)); W[nb][7] = ldstr(a3(n, 7)); }
      }
      if (TYPE && !UPL && ic.value < 16) {
        constexpr int p0 = (ic.value & 15) * 3 / 2, p1 = ((ic.value & 15) + 1) * 3 / 2;
        hpair(p0, 32 + 3 * I.m);
        if (p1 - p0 == 2) hpair(p0 + 1, 32 + 3 * I.m);
      }
      if (!TYPE && !UPQ && ic.value < 16) ppair(ic.value & 15);
      if (!TYPE && !OUP && ic.value == 16) {
        gstore();
      }
      if (XE && gi == 28) {
        constexpr int T = t >> 1, q = (t & 1) * 2;
#pragma unroll
        for (int j = q; j < q + 2; ++j)
          __builtin_amdgcn_raw_buffer_store_b128(u4_t{pk(acc[T][4 * j], acc[T][4 * j + 1]), rtag, pk(acc[T][4 * j + 2], acc[T][4 * j + 3]), rtag}, ws, xv + j * 1024, xs + T * 4096, SC1);
      }
      if (!XE && gi >= 28 && gi < 30) {
        constexpr int T = gi & 1;
        __builtin_amdgcn_raw_buffer_store_b128(u4_t{pk(acc[T][4 * t], acc[T][4 * t + 1]), rtag, pk(acc[T][4 * t + 2], acc[T][4 * t + 3]), rtag}, ws, xv + t * 1024, xs + T * 4096, SC1);
      }
      if (gi == XLG && t == 0) { const unsigned xa_ = xaddr(I, w * 64 + lane); X[0] = __builtin_amdgcn_raw_buffer_load_b128(ws, xa_, 0, SC1); X[1] = __builtin_amdgcn_raw_buffer_load_b128(ws, xa_ + 512, 0, SC1); }
      if (t == 3) {
        if (!TYPE && !OUP && gi == OGI) { ocomp(); SB(); STAMP(22); }
        if (gi == (TYPE ? LFG : OGI + OFD)) { vmcnt<TYPE ? nvm(UPL ? 0 : 3, LFG) : nvm(OGI, OGI + OFD) + (HB >= 0 ? 2 * (PDIST - HB) : 0)>(); __builtin_amdgcn_raw_buffer_store_b32(rtag, ws, l0, WS_HF + ptile * FST + I.m * 4 + (TYPE ? 128 + I.rh * FRH : 256), SC1); }
        if (gi == hissue(16)) waitflag(dws, fv, fs + 128, fval, rtag);
        if (gi == hissue(28)) waitflag(dws, fv2, fs2, fval, rtag);
        if (gi == hissue(16) - 3) fval = ld32(ws, fv, fs + 128);
        if (gi == hissue(28) - 3) fval = ld32(ws, fv2, fs2);
        sfor<36>([&](auto vc) { if (hissue(vc.value) == gi) hload(vc); });
        if (gi == hissue(35)) gbits = ld32(ws, lane * 4, WS_G + (ctile * 8 + I.c) * 256);
      }
      SB();
    }
  });
  STAMP(27);
  if (XLG == 36) { const unsigned xa_ = xaddr(I, w * 64 + lane); X[0] = __builtin_amdgcn_raw_buffer_load_b128(ws, xa_, 0, SC1); X[1] = __builtin_amdgcn_raw_buffer_load_b128(ws, xa_ + 512, 0, SC1); }
  SB();
  const float g = __builtin_bit_cast(float, gbits), g1 = keep ? 1.f - g : 0.f;
  if (!keep) { acc[0] = zero; acc[1] = zero; }
  *(AS3 u2_t*)(L_RED + ((keep ? 0 : 64) + (w & 1) * 32 + row) * 272 + 256) = u2_t{__builtin_bit_cast(unsigned, g1), rtag};
#pragma unroll
  for (int T = 0; T < 2; ++T)
#pragma unroll
    for (int q = 0; q < 4; ++q) {
      f4_t o;
#pragma unroll
      for (int j = 0; j < 4; ++j) { float y = g * acc[2 + T][4 * q + j]; asm("" : "+v"(y)); o[j] = __builtin_fmaf(g1, acc[T][4 * q + j], y); }
      *(AS3 f4_t*)(L_RED + ((w >> 1) * 64 + (w & 1) * 32 + row) * 272 + T * 128 + q * 32 + h * 16) = o;
    }
  SB();
  if (XLG == 37) { const unsigned xa_ = xaddr(I, w * 64 + lane); X[0] = __builtin_amdgcn_raw_buffer_load_b128(ws, xa_, 0, SC1); X[1] = __builtin_amdgcn_raw_buffer_load_b128(ws, xa_ + 512, 0, SC1); }
}

__global__ __launch_bounds__(512, 2) void fused_block(Params P) {
  __shared__ __attribute__((aligned(1024))) char lds[163840];
  asm volatile("" ::"v"((AS3 char*)lds) : "memory");
  unsigned long long pc;
  unsigned pf;
  asm volatile("s_getpc_b64 %0" : "=s"(pc));
  asm volatile("global_load_dword %0, %1, %2" : "=v"(pf) : "v"(min(PFA, CODE_BYTES)), "s"(pc & ~63ull) : "memory");
  SB();
  const int b = blockIdx.x, xcd = b & 7, s = b >> 3, il = s >> 3, jl = s & 7;
  Ids I;
  I.i = (xcd >> 1) * 4 + il; I.m = (xcd & 1) * 4 + (jl >> 1); I.c = jl; I.rh = xcd & 1; I.rot = ROTX; I.r2 = ROT2;
  const int w = __builtin_amdgcn_readfirstlane(threadIdx.x >> 6), lane = threadIdx.x & 63;
  const rsrc_t ws = __builtin_amdgcn_make_buffer_rsrc(P.ws, 0, WS_END, 0x00020000);
  unsigned prof = 0;
  RSTAMP(30);
  u4_t X[2];
  if (w >= 4) {
    if (jl & 1) loader<1>(P, I, w - 4, lane, pf, ws, X, prof); else loader<0>(P, I, w - 4, lane, pf, ws, X, prof);
  } else {
    if (jl & 1) compute<1>(P, I, w, lane, pf, ws, X, prof); else compute<0>(P, I, w, lane, pf, ws, X, prof);
  }
  bar();
  const int t = threadIdx.x >> 3, cq = threadIdx.x & 7;
  const unsigned xa_ = xaddr(I, threadIdx.x);
  const unsigned a = L_RED + t * 272 + cq * 32;
  f4_t v0 = ldsf4(a) + ldsf4(a + 64 * 272), v1 = ldsf4(a + 16) + ldsf4(a + 64 * 272 + 16);
  const u2_t gt = *(AS3 u2_t*)(L_RED + t * 272 + 256);
  for (int n = 0; n < SPIN_MAX; ++n) {
    if (__builtin_amdgcn_ballot_w64(((X[0][1] ^ gt[1]) | (X[0][3] ^ gt[1]) | (X[1][1] ^ gt[1]) | (X[1][3] ^ gt[1])) != 0) == 0) break;
    X[0] = __builtin_amdgcn_raw_buffer_load_b128(ws, xa_, 0, SC1);
    X[1] = __builtin_amdgcn_raw_buffer_load_b128(ws, xa_ + 512, 0, SC1);
    asm volatile("" ::: "memory");
  }
  STAMP(23);
  const float g1 = __builtin_bit_cast(float, gt[0]);
  v0 += g1 * f4_t{bflo(X[0][0]), bfhi(X[0][0]), bflo(X[0][2]), bfhi(X[0][2])};
  v1 += g1 * f4_t{bflo(X[1][0]), bfhi(X[1][0]), bflo(X[1][2]), bfhi(X[1][2])};
  __builtin_amdgcn_raw_buffer_store_b128(u4_t{pk(v0[0], v0[1]), pk(v0[2], v0[3]), pk(v1[0], v1[1]), pk(v1[2], v1[3])}, __builtin_amdgcn_make_buffer_rsrc(P.out, 0, 2048 * 1024, 0x00020000),
                                         t * 1024 + cq * 16, (I.i * 128 + I.rh * 64) * 1024 + I.c * 128, OAUX);
  if (threadIdx.x == 0 && I.c == 0 && I.rh == 0) {
    __builtin_amdgcn_raw_buffer_store_b32(gt[1], ws, 0, WS_EP + I.i * 128, SC1);
    if (I.i == 0) __builtin_amdgcn_raw_buffer_store_b32(*(AS3 unsigned*)L_TAG, ws, 0, WS_EP + 2048, SC1);
  }
  STAMP(28);
  RSTAMP(31);
}

extern "C" void kernel_launch(void* const* d_in, const int* in_sizes, int n_in, void* d_out, int out_size, void* d_ws,
                              size_t ws_size, hipStream_t stream) {
  Params P;
  P.x = (const char*)d_in[0]; P.wqkv = (const char*)d_in[1]; P.wo = (const char*)d_in[2]; P.w1 = (const char*)d_in[4];
  P.b1 = (const char*)d_in[5]; P.w2 = (const char*)d_in[6]; P.b2 = (const char*)d_in[7]; P.gw = (const char*)d_in[8];
  P.out = (char*)d_out; P.ws = (char*)d_ws;
  fused_block<<<256, 512, 0, stream>>>(P);
}
```

```cpp
#include <hip/hip_runtime.h>
#include <utility>

typedef __attribute__((ext_vector_type(8))) __bf16 bf8_t;
typedef __attribute__((ext_vector_type(4))) __bf16 bf4_t;
typedef __attribute__((ext_vector_type(2))) __bf16 bf2_t;
typedef __attribute__((ext_vector_type(16))) float f16_t;
typedef __attribute__((ext_vector_type(4))) float f4_t;
typedef __attribute__((ext_vector_type(2))) float f2_t;
typedef __attribute__((ext_vector_type(4))) unsigned u4_t;
typedef __attribute__((ext_vector_type(2))) unsigned u2_t;
typedef __amdgpu_buffer_rsrc_t rsrc_t;
#define AS3 __attribute__((address_space(3)))
#define DI __device__ __forceinline__
#define SB() __builtin_amdgcn_sched_barrier(0)
#define MFMA(a, b, c) __builtin_amdgcn_mfma_f32_32x32x16_bf16((a), (b), (c), 0, 0, 0)
#define SC1 16
#define HST SC1
#define HLD SC1

constexpr unsigned WS_KVP = 0x900000, WS_G = 0x980000, WS_HF = 0x9C0000, WS_KVF = 0x9A8000, WS_EP = 0x9B0000,
                   WS_X = 0xB00000, WS_END = 0xF00000;
constexpr unsigned SLOT = 16384, MISC = 9 * SLOT, L_GATE = MISC, L_B1 = MISC + 1024, L_B2 = MISC + 2048,
                   L_ZS = MISC + 2560, L_KVF = MISC + 4096, L_ZF = MISC + 6144, L_TAG = MISC + 6400, L_DUMP = MISC + 8192, L_KVS = 8 * SLOT, L_RED = 6 * SLOT;
#define CB 45696
constexpr unsigned CODE_BYTES = CB;
constexpr int SPIN_MAX = 1 << 17;
#define PDIST 8
#define ROT2 (il + jl)
#define ROTX ((2 * il + jl) & 7)
constexpr int NSTEP = 21;
#define UT1 {7, 9, 11, 13, 15, 17, 18, 20, 20, 22, 24, 25, 26, 27, 28, 29, 30, 31, 32, 32, 32}
#define UT0 {7, 9, 11, 13, 15, 18, 19, 21, 23, 23, 24, 24, 25, 26, 28, 29, 30, 31, 32, 32, 32}
#define SLP 10
#define PRIO 2
#define LGI 9
#define OFG 23
#define XLG 35
#define EFK 14
#define UPF 16
#define UPL 0
#define UPA 1
#define UPQ 1
#define KP (UPA ? 9 : 10)
#define HSP 1
#define FRH 384
#define FST 768
#define PSL "4"
#define SL0 0
#define XSL 8
#define HB -1
#define OLD SC1
#define KFK 4
#define OAUX SC1
#define XE 0
#define PFA ((blockIdx.x & 8) * 2048u + threadIdx.x * 64u)
#define KT 10
#define KS 10
#define LFG 5
#define OFD 3
#define LPRIO 3
constexpr bool OUP = KS == 10;
constexpr int OGI = OUP ? 0 : 4 * (KS - 11);
#define UP0 3
constexpr int UPRO = UP0;
constexpr int uafter(int type, int t) {
  constexpr int U1[NSTEP] = UT1;
  constexpr int U0[NSTEP] = UT0;
  return t < 0 ? UPRO : type ? U1[t] : U0[t];
}
constexpr int uneed(int type, int t) {
  return t < 8 ? 2 * t + 1 : t == 8 ? 17 : t == 9 ? 18 : t == 10 ? (type ? 20 : 21) : t == 11 ? 21 : t < 19 ? 10 + t : 30 + 2 * (t - 19);
}
constexpr bool uskip(int type, int u) { return type == 0 && (u == 17 || u == 20); }
constexpr int vmwait(int type, int t) {
  int n = 0;
  for (int u = uneed(type, t) + 1; u <= uafter(type, t - 1); ++u) n += uskip(type, u) ? 0 : 4;
  if (type == 0 && KS > KT && t == KS) n += 12;
  return n;
}
constexpr int imax(int a, int b) { return a > b ? a : b; }
constexpr int hissue(int v) { return v < 16 ? v - PDIST : v < 28 ? imax(v - PDIST, LGI + (v - 16) / 2) : imax(v - PDIST, OFG + (v - 28) / 2); }
constexpr int nvm(int g0, int g1) { int n = 0; for (int v = 0; v < 36; ++v) n += hissue(v) >= g0 && hissue(v) < g1 ? 2 : 0; return n; }
constexpr unsigned slot(int u) { return (unsigned)(u % 9) * SLOT; }
constexpr unsigned slo(int u) { return slot(u) & 0xffffu; }
constexpr unsigned shi(int u) { return slot(u) & ~0xffffu; }
constexpr int ufull(int ks) { return ks < 8 ? 16 : ks < 16 ? 18 : ks < 24 ? 19 : 21; }

struct Params {
  const char *x, *wqkv, *wo, *w1, *b1, *w2, *b2, *gw;
  char *out, *ws;
};
struct Ids { int i, m, c, rh, rot, r2; };
DI unsigned xaddr(const Ids& I, unsigned v) { asm volatile("" : "+v"(v)); return WS_X + (I.i * 8 + I.c) * 32768 + (2 * I.rh + (v >> 8)) * 8192 + (v & 7) * 1024 + ((v >> 3) & 31) * 16; }

template <int N> DI void vmcnt() { asm volatile("s_waitcnt vmcnt(%0)" ::"n"(N) : "memory"); }
DI void bar() { asm volatile("s_waitcnt lgkmcnt(0)\n\ts_barrier" ::: "memory"); SB(); }
DI bf8_t lds128(unsigned a) { return *(AS3 bf8_t*)a; }
DI f4_t ldsf4(unsigned a) { return *(AS3 f4_t*)a; }
DI bf4_t ldstr(unsigned a) { return __builtin_amdgcn_ds_read_tr16_b64_v4bf16((AS3 bf4_t*)a); }
DI bf8_t cat(bf4_t a, bf4_t b) { return __builtin_shufflevector(a, b, 0, 1, 2, 3, 4, 5, 6, 7); }
DI unsigned pk(float a, float b) { f2_t f = {a, b}; return __builtin_bit_cast(unsigned, __builtin_convertvector(f, bf2_t)); }
DI float bflo(unsigned v) { return __builtin_bit_cast(float, v << 16); }
DI float bfhi(unsigned v) { return __builtin_bit_cast(float, v & 0xffff0000u); }
DI u4_t pk8(const f16_t& a, int s) { return u4_t{pk(a[8 * s], a[8 * s + 1]), pk(a[8 * s + 2], a[8 * s + 3]), pk(a[8 * s + 4], a[8 * s + 5]), pk(a[8 * s + 6], a[8 * s + 7])}; }
DI bf8_t asbf(u4_t v) { return __builtin_bit_cast(bf8_t, v); }
DI float relu(float t) { return __builtin_bit_cast(float, max(__builtin_bit_cast(int, t), 0)); }
DI float phi(float t) {
  float e = __builtin_amdgcn_fmed3f(__builtin_amdgcn_exp2f(t * 1.44269504088896f), 0.f, 1.f);
  return (relu(t) + e) + 1e-6f;
}
DI float phi1(float t1) {
  float e = __builtin_amdgcn_fmed3f(__builtin_amdgcn_exp2f(__builtin_fmaf(t1, 1.44269504088896f, -1.44269504088896e-6f)), 0.f, 1.f);
  return __builtin_bit_cast(float, max(__builtin_bit_cast(int, t1), __builtin_bit_cast(int, 1e-6f))) + e;
}
typedef __attribute__((ext_vector_type(2))) short s2_t;
DI unsigned relu2pk(float a, float b) {
  float x = a * __builtin_fabsf(a);
  asm("" : "+v"(x));
  const s2_t p = __builtin_bit_cast(s2_t, pk(x, b * __builtin_fabsf(b)));
  return __builtin_bit_cast(unsigned, __builtin_elementwise_max(p, s2_t{0, 0}));
}
DI u4_t mkdesc(const void* p, unsigned bytes) {
  unsigned long long a = (unsigned long long)p;
  return u4_t{(unsigned)a, (unsigned)(a >> 32) & 0xffffu, bytes, 0x00020000u};
}
DI void dma(unsigned ldsa, unsigned voff, u4_t d, unsigned soff) {
  asm volatile("s_mov_b32 m0, %0\n\ts_nop 0\n\tbuffer_load_dwordx4 %1, %2, %3 offen lds" ::"s"(ldsa), "v"(voff), "s"(d), "s"(soff) : "memory");
}
DI unsigned ld32(rsrc_t r, unsigned v, unsigned s) { return __builtin_amdgcn_raw_buffer_load_b32(r, v, s, SC1); }
DI void waitflag(u4_t d, unsigned v, unsigned s, unsigned val, unsigned tag) {
  unsigned n = SPIN_MAX;
  asm volatile("s_nop 4\n1%=:\n\tv_cmp_ne_u32 vcc, %4, %0\n\ts_cbranch_vccz 2%=f\n\ts_sleep " PSL "\n\tbuffer_load_dword %0, %2, %3, %5 offen sc1\n\t"
               "s_sub_u32 %1, %1, 1\n\ts_waitcnt vmcnt(0)\n\ts_cbranch_scc0 1%=b\n2%=:"
               : "+v"(val), "+s"(n) : "v"(v), "s"(d), "s"(tag), "s"(s) : "memory", "vcc", "scc");
}
template <class F, int... T> DI void sfor_(F&& f, std::integer_sequence<int, T...>) { (f(std::integral_constant<int, T>{}), ...); }
template <int N, class F> DI void sfor(F&& f) { sfor_(f, std::make_integer_sequence<int, N>{}); }
#define PIN(x) asm volatile("" : "+v"(x))
template <int FROM, int TO> DI void mv(unsigned& a) { if (shi(FROM) != shi(TO)) { a += shi(TO) - shi(FROM); PIN(a); } }
constexpr unsigned OOB = 0x80000000u;
DI int unit_tile(int u, int c, int rh) { return u < 4 ? 16 * rh + 4 * ((u + c) & 3) : u < 7 ? 32 + 12 * rh + 4 * ((u - 4 + c) % 3) : 56 + 4 * ((u - 7 + c) & 3); }
constexpr int gunit(int gi) { return gi < 28 ? gi >> 2 : 7 + ((gi - 28) >> 1); }

template <int TYPE> DI void loader(const Params& P, const Ids& I, int lw, int lane, unsigned pf, rsrc_t ws, u4_t* X) {
  __builtin_amdgcn_s_setprio(LPRIO);
  constexpr unsigned WSTR = TYPE ? 3584 : 3072;
  const u4_t dx = mkdesc(P.x, 2048 * 1024), dw = TYPE ? mkdesc(P.w1, 512 * 3584) : mkdesc(P.wqkv, 512 * 3072);
  const u4_t d2 = mkdesc(P.w2, 1792 * 1024), dwo = mkdesc(P.wo, 512 * 1024);
  const unsigned r8 = lane >> 3, ch = lane & 7, sw = (ch ^ (((lane >> 4) & 1) << 2)) * 16;
  const unsigned vx0 = r8 * 1024 + (ch ^ (lane >> 4)) * 16, vx1 = r8 * 1024 + (ch ^ (4 + (lane >> 4))) * 16;
  const unsigned vw = r8 * WSTR + sw, vh = (lane >> 2) * WSTR + (lane & 3) * 16, v2 = r8 * 1024 + sw;
  const unsigned mo = I.m & 1;
  const unsigned xb = (I.i * 128 + lw * 32) * 1024;
  const unsigned c1 = TYPE ? 448 * I.m + 64 * mo + 128 * (lw >> 1) : 1024 * (1 + (lw >> 1)) + 128 * I.m;
  const unsigned cF = TYPE ? 448 * I.m + 64 * mo + 256 : 128 * I.m, cH = 448 * I.m + (mo ? 0 : 384);
  const unsigned lb = lw * 4096;
  const u4_t dws = mkdesc(P.ws, WS_END);
  const unsigned kfv = (lane & 15) * 4, kfo = WS_KVF + (I.m * 4 + lw) * 128;
  unsigned atag;
  u4_t gk[8];
  float gz[4];
  auto issue = [&](int u) {
    if (uskip(TYPE, u)) return;
    const unsigned l0 = slot(u) + lb;
    if (u < 16) {
      const unsigned kc = ((u >> 1) + I.rot) & 7;
      if ((u & 1) == 0) {
#pragma unroll
        for (int p = 0; p < 4; ++p) dma(l0 + p * 1024, (p & 1) ? vx1 : vx0, dx, xb + kc * 128 + p * 8192);
      } else {
#pragma unroll
        for (int p = 0; p < 4; ++p) dma(l0 + p * 1024, vw, dw, (kc * 64 + (lw & 1) * 32 + p * 8) * WSTR + c1);
      }
    } else if (u < 22) {
      if (u == 17 || u == 20) {
        const unsigned kc = ((u == 17 ? 0 : 4) + lw + I.rot) & 7;
#pragma unroll
        for (int p = 0; p < 4; ++p) dma(l0 + p * 1024, vh, dw, (kc * 64 + p * 16) * WSTR + cH);
      } else {
        const int n = u == 16 ? 0 : u == 18 ? 1 : u == 19 ? 2 : 3;
        const unsigned kc = (2 * n + (lw >> 1) + I.rot) & 7;
#pragma unroll
        for (int p = 0; p < 4; ++p) dma(l0 + p * 1024, vw, dw, (kc * 64 + (lw & 1) * 32 + p * 8) * WSTR + cF);
      }
    } else {
      const int q = u - 22;
      const unsigned ts = unit_tile(q, I.r2, I.rh) + lw;
      unsigned row;
      if (q < 4) { const unsigned mp = ts >> 2; row = 224 * mp + 32 * (mp & 1) + 32 * lw; }
      else if (q < 7) {
        const unsigned tt = ts - 32, mp = (tt * 43) >> 7, t3 = tt - 3 * mp;
        row = 224 * mp + ((mp & 1) ? (t3 < 2 ? 160 + 32 * t3 : 0) : 128 + 32 * t3);
      } else row = 32 * (ts - 56);
#pragma unroll
      for (int p = 0; p < 4; ++p) dma(l0 + p * 1024, v2, q < 7 ? d2 : dwo, (row + p * 8) * 1024 + I.c * 128);
    }
  };
#pragma unroll
  for (int u = 0; u <= UPRO; ++u) { if (u == 2 && SL0) __builtin_amdgcn_s_sleep(SL0); issue(u); }
  auto step = [&](auto tc) {
    constexpr int t = decltype(tc)::value;
    if (TYPE == 0 && t == 11) return;
    vmcnt<vmwait(TYPE, t)>();
    if (t == 0) asm volatile("" ::"v"(pf));
    bar();
    const unsigned pb = WS_KVP + I.m * 65536;
    if (TYPE == 0 && t == 0) atag = __builtin_amdgcn_readfirstlane(*(AS3 unsigned*)L_TAG);
    if (TYPE == 0 && t == KT) {
      waitflag(dws, kfv, kfo, atag + 1, atag);
      const unsigned gv = lw * 512 + (lane & 31) * 16 + (lane >> 5) * 32768, zv = 2048 + lw * 64 + (lane >> 4) * 4096 + (lane & 15) * 4;
#pragma unroll
      for (int r = 0; r < 8; ++r) asm volatile("buffer_load_dwordx4 %0, %1, %2, %3 offen sc1" : "=v"(gk[r]) : "v"(gv), "s"(dws), "s"(pb + r * 4096) : "memory");
#pragma unroll
      for (int r = 0; r < 4; ++r) asm volatile("buffer_load_dword %0, %1, %2, %3 offen sc1" : "=v"(gz[r]) : "v"(zv), "s"(dws), "s"(pb + r * 16384) : "memory");
    }
    if (TYPE && (t == 8 || t == 12)) __builtin_amdgcn_s_sleep(SLP);
#pragma unroll
    for (int u = uafter(TYPE, t - 1) + 1; u <= uafter(TYPE, t); ++u) issue(u);
    if (TYPE == 0 && t == KS) {
      vmcnt<4 * (uafter(0, KS) - uafter(0, KT - 1))>();
      float sm[8] = {};
#pragma unroll
      for (int r = 0; r < 8; ++r) {
        PIN(gk[r]);
#pragma unroll
        for (int k = 0; k < 8; ++k) sm[k] += k & 1 ? bfhi(gk[r][k >> 1]) : bflo(gk[r][k >> 1]);
      }
#pragma unroll
      for (int k = 0; k < 4; ++k) {
        asm volatile("s_nop 4\n\tv_permlane32_swap_b32 %0, %1\n\ts_nop 1" : "+v"(sm[k]), "+v"(sm[4 + k]));
        sm[k] += sm[4 + k];
      }
      *(AS3 u2_t*)(L_KVF + (lw >> 1) * 1024 + (2 * (lane & 31) + (lane >> 5)) * 16 + (lw & 1) * 8) = u2_t{pk(sm[0], sm[1]), pk(sm[2], sm[3])};
#pragma unroll
      for (int r = 0; r < 4; ++r) PIN(gz[r]);
      float z = (gz[0] + gz[1]) + (gz[2] + gz[3]);
      z += __shfl_xor(z, 16);
      z += __shfl_xor(z, 32);
      *(AS3 unsigned short*)(L_ZF + lw * 32 + (lane & 15) * 2) = (unsigned short)pk(z, z);
    }
  };
  sfor<NSTEP>(step);
  __builtin_amdgcn_s_sleep(XSL);
  const unsigned xa_ = xaddr(I, lw * 64 + 256 + lane);
  X[0] = __builtin_amdgcn_raw_buffer_load_b128(ws, xa_, 0, SC1);
  X[1] = __builtin_amdgcn_raw_buffer_load_b128(ws, xa_ + 512, 0, SC1);
}

template <int TYPE> DI void compute(const Params& P, const Ids& I, int w, int lane, unsigned pf, rsrc_t ws, u4_t* X) {
  __builtin_amdgcn_s_setprio(PRIO);
  const int tid = w * 64 + lane, h = lane >> 5, mo = I.m & 1;
  const unsigned e0 = ld32(ws, 0, WS_EP + I.i * 128), e1 = ld32(ws, 0, WS_EP + 2048);
  const unsigned m0 = TYPE ? *(const unsigned short*)(P.b1 + (224 * I.m + (mo ? (tid < 192 ? 32 + tid : tid - 192) : min(tid, 223))) * 2)
                           : *(const unsigned*)(P.gw + ((tid * 4 + I.rot * 128) & 1023));
  const unsigned m1 = *(const unsigned short*)(P.b2 + (64 * I.c + lane) * 2);
  SB();
  const unsigned r4 = (lane >> 2) & 3, trc = ((lane >> 4) & 1) * 32 + (lane & 3) * 8, tsw = (r4 >> 1) & 1;
  unsigned tA0 = h * 1024 + r4 * 128 + 64 * tsw + trc, tA1 = h * 1024 + r4 * 128 + 64 * (tsw ^ 1) + trc;
  unsigned tH = h * 512 + r4 * 64 + trc + shi(17), mb = MISC + h * 16;
  PIN(mb);
  const unsigned row = lane & 31;
  unsigned xa[4];
#pragma unroll
  for (int s = 0; s < 4; ++s) xa[s] = w * 4096 + row * 128 + ((2 * s + h) ^ ((row >> 1) & 7)) * 16;
  const unsigned ptile = I.i * 4 + w;
  const unsigned pv = ptile * (72 * 2048) + lane * 16;
  const unsigned ctile = I.i * 4 + I.rh * 2 + (w & 1);
  const unsigned cv = ctile * (72 * 2048) + (w >> 1) * 4096 + lane * 16;
  auto bias = [&](unsigned a) {
    f16_t r;
#pragma unroll
    for (int q = 0; q < 4; ++q) { f4_t b = ldsf4(mb + (a - MISC) + q * 32); r[4 * q] = b[0]; r[4 * q + 1] = b[1]; r[4 * q + 2] = b[2]; r[4 * q + 3] = b[3]; }
    return r;
  };
  const f16_t zero = {};
  const f16_t eps = zero + 1e-6f;
  const u4_t dws = mkdesc(P.ws, WS_END);
  const unsigned l0 = lane ? OOB : 0;
  SB();
  *(AS3 unsigned*)((TYPE ? L_B1 : L_GATE) + tid * 4) = TYPE ? m0 << 16 : m0;
  *(AS3 unsigned*)(L_B2 + lane * 4) = m1 << 16;
  const unsigned rtag = __builtin_amdgcn_readfirstlane(e0) + 1, atag = __builtin_amdgcn_readfirstlane(e1) + 1;
  if (!TYPE) { *(AS3 unsigned*)L_TAG = atag; *(AS3 unsigned*)(L_B1 + tid * 4) = 0; }
  asm volatile("" ::"s"(rtag), "s"(atag), "v"(pf));
  bar();
  bf8_t xf[32];
  bf4_t W[2][8];
  f16_t acc[4];
  auto a1 = [&](int ks, int r) { return slo(2 * (ks >> 2) + 1) + (r >> 2) * 8192 + (ks & 3) * 2048 + (r & 1) * 512 + ((r & 2) ? tA1 : tA0); };
  xf[0] = lds128(slot(0) + xa[0]);
#pragma unroll
  for (int r = 0; r < 8; ++r) W[0][r] = ldstr(a1(0, r));
#pragma unroll
  for (int t = 0; t < 4; ++t) acc[t] = TYPE ? bias(L_B1 + t * 128) : t < 2 ? eps : zero;
  SB();
  sfor<128>([&](auto ic) {
    constexpr int ks = ic.value >> 2, t = ic.value & 3, cb = ks & 1, nb = cb ^ 1, n = ks + 1;
    {
      const bf8_t wf = cat(W[cb][2 * t], W[cb][2 * t + 1]);
      acc[t] = TYPE ? MFMA(wf, xf[ks], acc[t]) : MFMA(xf[ks], wf, acc[t]);
      PIN(acc[t]);
      SB();
      if (t == 0 && (ks & 3) == 3) { bar(); }
      if (n < 32) {
        if (t == 0) {
          if (n >= 4) mv<2 * (n >> 2) - 2, 2 * (n >> 2)>(xa[n & 3]);
          if ((n & 3) == 0) { mv<2 * (n >> 2) - 1, 2 * (n >> 2) + 1>(tA0); mv<2 * (n >> 2) - 1, 2 * (n >> 2) + 1>(tA1); }
        }
        if (t == 0) { xf[n & 31] = lds128(slo(2 * (n >> 2)) + xa[n & 3]); W[nb][0] = ldstr(a1(n, 0)); W[nb][1] = ldstr(a1(n, 1)); }
        if (t == 1) { W[nb][2] = ldstr(a1(n, 2)); W[nb][3] = ldstr(a1(n, 3)); W[nb][4] = ldstr(a1(n, 4)); }
        if (t == 2) { W[nb][5] = ldstr(a1(n, 5)); W[nb][6] = ldstr(a1(n, 6)); W[nb][7] = ldstr(a1(n, 7)); }
        SB();
      }
    }
  });
  f16_t old[4];
  u4_t E[8];
#pragma unroll
  for (int t = 0; t < 4; ++t) old[t] = acc[t];
  auto hpair = [&](int p, unsigned ts0) {
    unsigned v = relu2pk(old[p >> 3][2 * (p & 7)], old[p >> 3][2 * (p & 7) + 1]);
    PIN(v);
    E[p >> 2][p & 3] = v;
    if ((p & 3) == 3) __builtin_amdgcn_raw_buffer_store_b128(E[p >> 2], ws, pv + ((p >> 2) & 1) * 1024, (ts0 + (p >> 3)) * 2048, HST);
  };
  auto ppair = [&](int p) {
    float x = phi1(old[p >> 3][2 * (p & 7)]);
    asm("" : "+v"(x));
    unsigned v = pk(x, phi1(old[p >> 3][2 * (p & 7) + 1]));
    PIN(v);
    E[p >> 2][p & 3] = v;
  };
  const bool hi = lane & 16;
  f16_t kv, kz;
  auto kvmm = [&](int T) {
    const bf8_t ones = asbf(u4_t{0x3f803f80u, 0x3f803f80u, 0x3f803f80u, 0x3f803f80u});
    kv = MFMA(asbf(E[2 * T]), asbf(E[4 + 2 * T]), zero);
    kz = MFMA(asbf(E[2 * T]), ones, zero);
    kv = MFMA(asbf(E[2 * T + 1]), asbf(E[5 + 2 * T]), kv);
    kz = MFMA(asbf(E[2 * T + 1]), ones, kz);
    PIN(kv); PIN(kz);
  };
  auto kvst = [&](int T, int q) {
    f4_t o;
#pragma unroll
    for (int j = 0; j < 4; ++j) { float lo_ = kv[4 * q + j]; PIN(lo_); o[j] = hi ? kv[8 + 4 * q + j] : lo_; }
    *(AS3 f4_t*)(L_KVS + (2 * T + q) * 4096 + tid * 16) = o;
    const unsigned za = row == 0 ? L_ZS + w * 256 + h * 64 : L_DUMP + tid * 16;
#pragma unroll
    for (int j = 2 * q; j < 2 * q + 2; ++j) *(AS3 f4_t*)(za + T * 128 + j * 16) = f4_t{kz[4 * j], kz[4 * j + 1], kz[4 * j + 2], kz[4 * j + 3]};
  };
  if (TYPE) {
#pragma unroll
    for (int p = 0; p < UPF; ++p) hpair(p, 4 * I.m);
  } else {
#pragma unroll
    for (int q = 0; q < 4; ++q) { E[4 + q] = pk8(old[2 + (q >> 1)], q & 1); PIN(E[4 + q]); }
    if (UPA) {
#pragma unroll
      for (int p = 0; p < 8; ++p) ppair(p);
      kvmm(0);
#pragma unroll
      for (int p = 8; p < 16; ++p) ppair(p);
      kvst(0, 0); kvst(0, 1);
      kvmm(1);
      kvst(1, 0); kvst(1, 1);
    }
  }
  auto a2 = [&](int ks, int r) {
    return r < 4 ? slo(ufull(ks)) + (ks & 7) * 2048 + (r & 1) * 512 + ((r & 2) ? tA1 : tA0) : slo(ks < 16 ? 17 : 20) + (ks & 15) * 1024 + (r & 1) * 256 + tH;
  };
  bf8_t G[2];
  mv<15, 16>(tA0); mv<15, 16>(tA1);
  PIN(tH);
#pragma unroll
  for (int r = 0; r < (TYPE ? 6 : 4); ++r) W[0][r] = ldstr(a2(0, r));
  unsigned gb = mb + ((((lane >> 4) ^ lane) & 1) ? L_B1 - L_GATE : 0);
  PIN(gb);
  f4_t ga = {};
  if (!TYPE) G[0] = lds128(gb);
#pragma unroll
  for (int t = 0; t < 3; ++t) acc[t] = TYPE ? bias(L_B1 + (4 + t) * 128) : eps;
  const unsigned fv = (lane & 3) * 4, fs = WS_HF + ptile * FST + I.rh * (16 + FRH), fv2 = (lane & 7) * 4, fs2 = WS_HF + ctile * FST + 256;
  unsigned fval = 0;
  u4_t H[36][2];
  auto hload = [&](auto gc) {
    constexpr int gi = gc.value;
    const unsigned so = (unit_tile(gunit(gi), I.r2, I.rh) + (gi < 28 ? gi & 2 : 0)) * 2048;
#pragma unroll
    for (int q = 0; q < 2; ++q) H[gi][q] = __builtin_amdgcn_raw_buffer_load_b128(ws, (gi < 28 ? pv : cv) + (gi & 1) * 2048 + q * 1024, so, gi < 28 ? HLD : OLD);
  };
  SB();
  sfor<96>([&](auto ic) {
    constexpr int ks = ic.value / 3, t = ic.value % 3, cb = ks & 1, nb = cb ^ 1, n = ks + 1;
    {
      if (TYPE || t < 2) { acc[t] = MFMA(cat(W[cb][2 * t], W[cb][2 * t + 1]), xf[ks], acc[t]); PIN(acc[t]); }
      else { ga = __builtin_amdgcn_mfma_f32_16x16x32_bf16(G[cb], xf[ks], ga, 0, 0, 0); PIN(ga); }
      SB();
      if (t == 0 && (TYPE ? (ks & 7) == 7 : (ks == (UPA ? 0 : 7) || ks == 11 || (!OUP && ks == 31)))) { bar(); }
      if (!TYPE && ic.value == 0) {
      f4_t s = ldsf4(L_KVS + w * 4096 + lane * 16);
      float z = *(AS3 float*)(L_ZS + w * 64 + (lane & 15) * 4);
#pragma unroll
      for (int q = 1; q < 4; ++q) { s += ldsf4(L_KVS + w * 4096 + q * 1024 + lane * 16); z += *(AS3 float*)(L_ZS + q * 256 + w * 64 + (lane & 15) * 4); }
      __builtin_amdgcn_raw_buffer_store_b64(u2_t{pk(s[0], s[1]), pk(s[2], s[3])}, ws, w * 512 + lane * 8, WS_KVP + I.m * 65536 + I.i * 4096, SC1);
      __builtin_amdgcn_raw_buffer_store_b32(__builtin_bit_cast(unsigned, z), ws, (lane > 15 ? OOB : 2048) + w * 64 + lane * 4, WS_KVP + I.m * 65536 + I.i * 4096, SC1);
      SB();
      }
      if (!TYPE && t == 2 && ks == KFK) { vmcnt<0>(); __builtin_amdgcn_raw_buffer_store_b32(atag, ws, l0, WS_KVF + (I.m * 4 + w) * 128 + I.i * 4, SC1); }
      if (n < 32) {
        if (t == 0) { mv<ufull(ks), ufull(n)>(tA0); mv<ufull(ks), ufull(n)>(tA1); if (TYPE && n == 16) mv<17, 20>(tH); }
        if (t == 0) { W[nb][0] = ldstr(a2(n, 0)); W[nb][1] = ldstr(a2(n, 1)); W[nb][2] = ldstr(a2(n, 2)); }
        if (t == 1) {
          W[nb][3] = ldstr(a2(n, 3));
          if (TYPE) { W[nb][4] = ldstr(a2(n, 4)); W[nb][5] = ldstr(a2(n, 5)); } else G[nb] = lds128(gb + n * 32);
        }
      }
      if (TYPE) {
        if (ic.value % HSP == 0 && ic.value / HSP < 32 - UPF) hpair((UPF + ic.value / HSP) & 31, 4 * I.m);
        if (t == 2 && ks == EFK) { vmcnt<0>(); __builtin_amdgcn_raw_buffer_store_b32(rtag, ws, l0, WS_HF + ptile * FST + I.m * 4 + I.rh * FRH, SC1); }
      } else if (!UPA && ks < 11) {
        if (t < 2 && ks < 4) ppair((2 * ks + t) & 15);
        if (t < 2 && ks >= 5 && ks < 9) ppair((2 * ks + t - 2) & 15);
        if (t == 0 && (ks == 4 || ks == 9)) kvmm(ks == 9);
        if (t == 2 && (ks == 4 || ks == 9)) kvst(ks == 9, 0);
        if (t == 2 && ks == 5) kvst(0, 1);
        if (t == 0 && ks == 10) kvst(1, 1);
      }
      if (t == 2 && (TYPE || HB < 0)) {
        if (ks == 27 - PDIST) fval = ld32(ws, fv, fs);
        if (ks == 32 - PDIST) { waitflag(dws, fv, fs, fval, rtag); }
        if (ks >= 32 - PDIST) hload(std::integral_constant<int, (ks + PDIST) & 31>{});
      }
      SB();
    }
  });
#pragma unroll
  for (int t = 0; t < 3; ++t) old[t] = acc[t];
  auto ocomp = [&]() {
    const u4_t z4 = {};
#pragma unroll
    for (int T = 0; T < 2; ++T) {
      const bf8_t p0 = asbf(E[2 * T]), p1 = asbf(E[2 * T + 1]);
      const u4_t kf = *(AS3 u4_t*)(L_KVF + T * 1024 + lane * 16);
      f16_t num = MFMA(asbf(hi ? z4 : kf), p0, zero);
      const f16_t d0 = MFMA(lds128(L_ZF + T * 64 + h * 32), p0, zero);
      num = MFMA(asbf(hi ? kf : z4), p1, num);
      const f16_t d1 = MFMA(lds128(L_ZF + T * 64 + h * 32 + 16), p1, zero);
      const float r0 = __builtin_amdgcn_rcpf(d0[0] + 1e-6f), r1 = __builtin_amdgcn_rcpf(d1[0] + 1e-6f);
#pragma unroll
      for (int r = 0; r < 16; ++r) num[r] *= r < 8 ? r0 : r1;
      __builtin_amdgcn_raw_buffer_store_b128(pk8(num, 0), ws, pv, (56 + 2 * I.m + T) * 2048, SC1);
      __builtin_amdgcn_raw_buffer_store_b128(pk8(num, 1), ws, pv + 1024, (56 + 2 * I.m + T) * 2048, SC1);
    }
  };
  auto gstore = [&]() {
        const float g = __builtin_amdgcn_rcpf(1.f + __builtin_amdgcn_exp2f((hi ? ga[1] : ga[0]) * -1.44269504088896f));
        __builtin_amdgcn_raw_buffer_store_b32(__builtin_bit_cast(unsigned, g), ws, lane * 4, WS_G + (ptile * 8 + I.m) * 256, SC1);
  };
  if (TYPE && UPL) {
#pragma unroll
    for (int p = 0; p < 24; ++p) hpair(p, 32 + 3 * I.m);
  }
  if (!TYPE && UPQ) {
#pragma unroll
    for (int p = 0; p < 16; ++p) ppair(p);
    if (OUP) {
      gstore();
      if (HB >= 0) { waitflag(dws, fv, fs, rtag + 1, rtag); }
      bar();
      sfor<PDIST>([&](auto vc) { if (vc.value < HB) hload(vc); });
      SB();
      ocomp();
      SB();
      sfor<PDIST>([&](auto vc) { if (HB >= 0 && vc.value >= HB) hload(vc); });
    }
  }
  unsigned tB0 = shi(22) + (h * 4 + r4) * 128 + 64 * tsw + trc, tB1 = tB0 ^ 64;
  PIN(tB0); PIN(tB1);
  auto a3 = [&](int gi, int r) { return slo(22 + gunit(gi)) + (gi & (gi < 28 ? 3 : 1)) * 4096 + (r >> 2) * 2048 + (r & 1) * 1024 + ((r & 2) ? tB1 : tB0); };
#pragma unroll
  for (int r = 0; r < 8; ++r) W[0][r] = ldstr(a3(0, r));
  const bool keep = (w >> 1) == I.rh;
  const unsigned xs = WS_X + (I.i * 8 + I.c) * 32768 + w * 8192;
  const unsigned xv = lane * 16 + (keep ? OOB : 0);
  acc[0] = bias(L_B2);
  acc[1] = bias(L_B2 + 128);
#pragma unroll
  for (int r = 0; r < 16; ++r) { acc[0][r] = keep ? acc[0][r] : 0.f; acc[1][r] = keep ? acc[1][r] : 0.f; }
  acc[2] = zero;
  acc[3] = zero;
  unsigned gbits = 0;
  SB();
  sfor<144>([&](auto ic) {
    constexpr int gi = ic.value >> 2, t = ic.value & 3, cb = gi & 1, nb = cb ^ 1, n = gi + 1, ab = gi < 28 ? 0 : 2;
    {
      acc[ab + (t & 1)] = MFMA(cat(W[cb][2 * t], W[cb][2 * t + 1]), asbf(H[gi][t >> 1]), acc[ab + (t & 1)]);
      PIN(acc[ab + (t & 1)]);
      SB();
      if (t == 0 && (gi & 3) == 3 && n < 36) { bar(); }
      if (n < 36) {
        if (t == 0) { mv<22 + gunit(gi), 22 + gunit(n)>(tB0); mv<22 + gunit(gi), 22 + gunit(n)>(tB1); }
        if (t == 0 && n == 28) { tB0 += (w >> 1) * 8192; tB1 += (w >> 1) * 8192; PIN(tB0); PIN(tB1); }
        if (t == 0) { W[nb][0] = ldstr(a3(n, 0)); W[nb][1] = ldstr(a3(n, 1)); W[nb][2] = ldstr(a3(n, 2)); }
        if (t == 1) { W[nb][3] = ldstr(a3(n, 3)); W[nb][4] = ldstr(a3(n, 4)); W[nb][5] = ldstr(a3(n, 5)); }
        if (t == 2) { W[nb][6] = ldstr(a3(n, 6)); W[nb][7] = ldstr(a3(n, 7)); }
      }
      if (TYPE && !UPL && ic.value < 16) {
        constexpr int p0 = (ic.value & 15) * 3 / 2, p1 = ((ic.value & 15) + 1) * 3 / 2;
        hpair(p0, 32 + 3 * I.m);
        if (p1 - p0 == 2) hpair(p0 + 1, 32 + 3 * I.m);
      }
      if (!TYPE && !UPQ && ic.value < 16) ppair(ic.value & 15);
      if (!TYPE && !OUP && ic.value == 16) {
        gstore();
      }
      if (XE && gi == 28) {
        constexpr int T = t >> 1, q = (t & 1) * 2;
#pragma unroll
        for (int j = q; j < q + 2; ++j)
          __builtin_amdgcn_raw_buffer_store_b128(u4_t{pk(acc[T][4 * j], acc[T][4 * j + 1]), rtag, pk(acc[T][4 * j + 2], acc[T][4 * j + 3]), rtag}, ws, xv + j * 1024, xs + T * 4096, SC1);
      }
      if (!XE && gi >= 28 && gi < 30) {
        constexpr int T = gi & 1;
        __builtin_amdgcn_raw_buffer_store_b128(u4_t{pk(acc[T][4 * t], acc[T][4 * t + 1]), rtag, pk(acc[T][4 * t + 2], acc[T][4 * t + 3]), rtag}, ws, xv + t * 1024, xs + T * 4096, SC1);
      }
      if (gi == XLG && t == 0) { const unsigned xa_ = xaddr(I, w * 64 + lane); X[0] = __builtin_amdgcn_raw_buffer_load_b128(ws, xa_, 0, SC1); X[1] = __builtin_amdgcn_raw_buffer_load_b128(ws, xa_ + 512, 0, SC1); }
      if (t == 3) {
        if (!TYPE && !OUP && gi == OGI) { ocomp(); SB(); }
        if (gi == (TYPE ? LFG : OGI + OFD)) { vmcnt<TYPE ? nvm(UPL ? 0 : 3, LFG) : nvm(OGI, OGI + OFD) + (HB >= 0 ? 2 * (PDIST - HB) : 0)>(); __builtin_amdgcn_raw_buffer_store_b32(rtag, ws, l0, WS_HF + ptile * FST + I.m * 4 + (TYPE ? 128 + I.rh * FRH : 256), SC1); }
        if (gi == hissue(16)) waitflag(dws, fv, fs + 128, fval, rtag);
        if (gi == hissue(28)) waitflag(dws, fv2, fs2, fval, rtag);
        if (gi == hissue(16) - 3) fval = ld32(ws, fv, fs + 128);
        if (gi == hissue(28) - 3) fval = ld32(ws, fv2, fs2);
        sfor<36>([&](auto vc) { if (hissue(vc.value) == gi) hload(vc); });
        if (gi == hissue(35)) gbits = ld32(ws, lane * 4, WS_G + (ctile * 8 + I.c) * 256);
      }
      SB();
    }
  });
  if (XLG == 36) { const unsigned xa_ = xaddr(I, w * 64 + lane); X[0] = __builtin_amdgcn_raw_buffer_load_b128(ws, xa_, 0, SC1); X[1] = __builtin_amdgcn_raw_buffer_load_b128(ws, xa_ + 512, 0, SC1); }
  SB();
  const float g = __builtin_bit_cast(float, gbits), g1 = keep ? 1.f - g : 0.f;
  if (!keep) { acc[0] = zero; acc[1] = zero; }
  *(AS3 u2_t*)(L_RED + ((keep ? 0 : 64) + (w & 1) * 32 + row) * 272 + 256) = u2_t{__builtin_bit_cast(unsigned, g1), rtag};
#pragma unroll
  for (int T = 0; T < 2; ++T)
#pragma unroll
    for (int q = 0; q < 4; ++q) {
      f4_t o;
#pragma unroll
      for (int j = 0; j < 4; ++j) { float y = g * acc[2 + T][4 * q + j]; asm("" : "+v"(y)); o[j] = __builtin_fmaf(g1, acc[T][4 * q + j], y); }
      *(AS3 f4_t*)(L_RED + ((w >> 1) * 64 + (w & 1) * 32 + row) * 272 + T * 128 + q * 32 + h * 16) = o;
    }
  SB();
  if (XLG == 37) { const unsigned xa_ = xaddr(I, w * 64 + lane); X[0] = __builtin_amdgcn_raw_buffer_load_b128(ws, xa_, 0, SC1); X[1] = __builtin_amdgcn_raw_buffer_load_b128(ws, xa_ + 512, 0, SC1); }
}

__global__ __launch_bounds__(512, 2) void fused_block(Params P) {
  __shared__ __attribute__((aligned(1024))) char lds[163840];
  asm volatile("" ::"v"((AS3 char*)lds) : "memory");
  unsigned long long pc;
  unsigned pf;
  asm volatile("s_getpc_b64 %0" : "=s"(pc));
  asm volatile("global_load_dword %0, %1, %2" : "=v"(pf) : "v"(min(PFA, CODE_BYTES)), "s"(pc & ~63ull) : "memory");
  SB();
  const int b = blockIdx.x, xcd = b & 7, s = b >> 3, il = s >> 3, jl = s & 7;
  Ids I;
  I.i = (xcd >> 1) * 4 + il; I.m = (xcd & 1) * 4 + (jl >> 1); I.c = jl; I.rh = xcd & 1; I.rot = ROTX; I.r2 = ROT2;
  const int w = __builtin_amdgcn_readfirstlane(threadIdx.x >> 6), lane = threadIdx.x & 63;
  const rsrc_t ws = __builtin_amdgcn_make_buffer_rsrc(P.ws, 0, WS_END, 0x00020000);
  u4_t X[2];
  if (w >= 4) {
    if (jl & 1) loader<1>(P, I, w - 4, lane, pf, ws, X); else loader<0>(P, I, w - 4, lane, pf, ws, X);
  } else {
    if (jl & 1) compute<1>(P, I, w, lane, pf, ws, X); else compute<0>(P, I, w, lane, pf, ws, X);
  }
  bar();
  const int t = threadIdx.x >> 3, cq = threadIdx.x & 7;
  const unsigned xa_ = xaddr(I, threadIdx.x);
  const unsigned a = L_RED + t * 272 + cq * 32;
  f4_t v0 = ldsf4(a) + ldsf4(a + 64 * 272), v1 = ldsf4(a + 16) + ldsf4(a + 64 * 272 + 16);
  const u2_t gt = *(AS3 u2_t*)(L_RED + t * 272 + 256);
  for (int n = 0; n < SPIN_MAX; ++n) {
    if (__builtin_amdgcn_ballot_w64(((X[0][1] ^ gt[1]) | (X[0][3] ^ gt[1]) | (X[1][1] ^ gt[1]) | (X[1][3] ^ gt[1])) != 0) == 0) break;
    X[0] = __builtin_amdgcn_raw_buffer_load_b128(ws, xa_, 0, SC1);
    X[1] = __builtin_amdgcn_raw_buffer_load_b128(ws, xa_ + 512, 0, SC1);
    asm volatile("" ::: "memory");
  }
  const float g1 = __builtin_bit_cast(float, gt[0]);
  v0 += g1 * f4_t{bflo(X[0][0]), bfhi(X[0][0]), bflo(X[0][2]), bfhi(X[0][2])};
  v1 += g1 * f4_t{bflo(X[1][0]), bfhi(X[1][0]), bflo(X[1][2]), bfhi(X[1][2])};
  __builtin_amdgcn_raw_buffer_store_b128(u4_t{pk(v0[0], v0[1]), pk(v0[2], v0[3]), pk(v1[0], v1[1]), pk(v1[2], v1[3])}, __builtin_amdgcn_make_buffer_rsrc(P.out, 0, 2048 * 1024, 0x00020000),
                                         t * 1024 + cq * 16, (I.i * 128 + I.rh * 64) * 1024 + I.c * 128, OAUX);
  if (threadIdx.x == 0 && I.c == 0 && I.rh == 0) {
    __builtin_amdgcn_raw_buffer_store_b32(gt[1], ws, 0, WS_EP + I.i * 128, SC1);
    if (I.i == 0) __builtin_amdgcn_raw_buffer_store_b32(*(AS3 unsigned*)L_TAG, ws, 0, WS_EP + 2048, SC1);
  }
}

extern "C" void kernel_launch(void* const* d_in, const int* in_sizes, int n_in, void* d_out, int out_size, void* d_ws,
                              size_t ws_size, hipStream_t stream) {
  Params P;
  P.x = (const char*)d_in[0]; P.wqkv = (const char*)d_in[1]; P.wo = (const char*)d_in[2]; P.w1 = (const char*)d_in[4];
  P.b1 = (const char*)d_in[5]; P.w2 = (const char*)d_in[6]; P.b2 = (const char*)d_in[7]; P.gw = (const char*)d_in[8];
  P.out = (char*)d_out; P.ws = (char*)d_ws;
  fused_block<<<256, 512, 0, stream>>>(P);
}
```
